```python
import torch

def _softmax(x, axis=-1):
    return torch.softmax(x, dim=axis)
def _gelu(x):
    return torch.nn.functional.gelu(x, approximate="tanh")
def _silu(x):
    return torch.nn.functional.silu(x)
def _sigmoid(x):
    return torch.sigmoid(x)
def _layernorm(x, axis=-1, eps=1e-5):
    m = x.mean(dim=axis, keepdim=True)
    v = x.var(dim=axis, keepdim=True, unbiased=False)
    return (x - m) / torch.sqrt(v + eps)
def _scatter_add(tgt, idx, src, axis=0):
    return tgt.clone().index_add_(axis, idx, src)
def _segment_sum(src, seg, n):
    out = torch.zeros((n,) + src.shape[1:], dtype=src.dtype)
    return out.index_add_(0, seg, src)
def _pad(x, pw):
    flat = tuple(v for pair in reversed(pw) for v in pair)
    return torch.nn.functional.pad(x, flat)

kernel_name = 'gated_mix__ssm_conv_h3__compressed_kv_cla__prompt'

def setup_inputs():

    _w = (lambda a: (lambda t: t.to(torch.bfloat16) if t.is_floating_point() else t)(torch.as_tensor(a)))
    def _A_setup():
        B, S, D = 1, 2048, 640
        ks = 4
        return {
            "q": _w((lambda *s: torch.randn(*s, dtype=torch.bfloat16) * 0.05)(B, S, D)),
            "k": _w((lambda *s: torch.randn(*s, dtype=torch.bfloat16) * 0.4)(B, S, D)),
            "v": _w((lambda *s: torch.randn(*s, dtype=torch.bfloat16) * 0.4)(B, S, D)),
            "shift_k": _w((lambda *s: torch.randn(*s, dtype=torch.bfloat16) * 0.4)(D, ks) * 0.5),
            "ssm_k": _w((lambda *s: torch.randn(*s, dtype=torch.bfloat16) * 0.8)(D, S) * (1.0 / S ** 0.5)),
            "ssm_D": _w((lambda *s: torch.randn(*s, dtype=torch.bfloat16) * 0.4)(D)),
        }
    _A_in = _A_setup()

    def _B_setup():
        B, H, Sq, Sk, D, M = 1, 32, 2048, 2048, 128, 640
        Dc = 4 * D
        return {
            "x": _w((lambda *s: torch.randn(*s, dtype=torch.bfloat16) * 0.05)(B, Sq, M)),
            "c_kv": _w((lambda *s: torch.randn(*s, dtype=torch.bfloat16) * 0.4)(B, Sk, Dc)),
            "w_q": _w((lambda *s: torch.randn(*s, dtype=torch.bfloat16) * 6.4)(M, H, D) * (M ** -0.5)),
            "w_uk": _w((lambda *s: torch.randn(*s, dtype=torch.bfloat16) * 6.4)(Dc, H, D) * (Dc ** -0.5)),
            "w_uv": _w((lambda *s: torch.randn(*s, dtype=torch.bfloat16) * 0.4)(Dc, H, D) * (Dc ** -0.5)),
            "w_o": _w((lambda *s: torch.randn(*s, dtype=torch.bfloat16) * 0.4)(H, D, M) * ((H * D) ** -0.5)),
        }
    _B_in = _B_setup()

    return {
        "A_q": _A_in["q"],
        "A_k": _A_in["k"],
        "A_v": _A_in["v"],
        "A_shift_k": _A_in["shift_k"],
        "A_ssm_k": _A_in["ssm_k"],
        "A_ssm_D": _A_in["ssm_D"],
        "B_x": _B_in["x"],
        "B_c_kv": _B_in["c_kv"],
        "B_w_q": _B_in["w_q"],
        "B_w_uk": _B_in["w_uk"],
        "B_w_uv": _B_in["w_uv"],
        "B_w_o": _B_in["w_o"],
        "gate_w": _w((lambda *s: torch.randn(*s, dtype=torch.bfloat16) * 0.05)(640) * 16.0),
    }

def reference(A_q, A_k, A_v, A_shift_k, A_ssm_k, A_ssm_D, B_x, B_c_kv, B_w_q, B_w_uk, B_w_uv, B_w_o, gate_w):
    def _A_ref(q, k, v, shift_k, ssm_k, ssm_D):
        B, S, D = q.shape
        ks = shift_k.shape[-1]
        k_pad = _pad(k, ((0, 0), (ks - 1, 0), (0, 0)))
        k_shift = torch.zeros_like(k)
        for t in range(ks):
            k_shift = k_shift + k_pad[:, t:t + S, :] * shift_k[:, ks - 1 - t][None, None, :]
        kv = k_shift * v
        L = 2 * S
        kv_pad = _pad(kv, ((0, 0), (0, S), (0, 0)))
        kf_pad = _pad(ssm_k, ((0, 0), (0, S)))
        kv_f = (lambda x, axes=(-2,-1): torch.fft.rfft2(x.float(), dim=axes))(kv_pad[:, None, :, :], axes=(1, 2))
        kf_f = (lambda x, axes=(-2,-1): torch.fft.rfft2(x.float(), dim=axes))(kf_pad[:, None, :], axes=(1, 2))
        kf_f = (lambda x, *d: x.permute(*d))(kf_f, 1, 2, 0)
        y_f = kv_f * kf_f[None, :, :, :]
        y = (lambda x, s, axes=(-2,-1): torch.fft.irfft2(x, s=s, dim=axes).to(torch.bfloat16))(y_f, (1, L), axes=(1, 2))[:, 0, :S, :]
        y = y + kv * ssm_D[None, None, :]
        return q * y

    def _B_ref(x, c_kv, w_q, w_uk, w_uv, w_o):
        B, Sq, M = x.shape[0], x.shape[1], x.shape[2]
        Sk = c_kv.shape[1]
        H, D = w_q.shape[1], w_q.shape[2]
        q = torch.einsum("bqm,mhd->bhqd", x, w_q)
        k = torch.einsum("bkc,chd->bhkd", c_kv, w_uk)
        v = torch.einsum("bkc,chd->bhkd", c_kv, w_uv)
        scores = torch.einsum("bhqd,bhkd->bhqk", q, k) * (D ** -0.5)
        off = Sk - Sq
        qi = torch.arange(Sq)[:, None]
        kj = torch.arange(Sk)[None, :]
        cmask = kj <= (qi + off)
        scores = torch.where(cmask[None, None], scores, -1e9)
        p = _softmax(scores, axis=-1)
        ctx = torch.einsum("bhqk,bhkd->bhqd", p, v)
        return torch.einsum("bhqd,hdm->bqm", ctx, w_o)

    x = A_q
    g = _sigmoid((x * gate_w).sum(-1, keepdims=True))
    a = _A_ref(x, A_k, A_v, A_shift_k, A_ssm_k, A_ssm_D)
    b = _B_ref(x, B_c_kv, B_w_q, B_w_uk, B_w_uv, B_w_o)
    return g * a + (1 - g) * b
```

```cpp
#include <hip/hip_runtime.h>
#include <stdint.h>

#define DI __device__ __forceinline__
#define LAS __attribute__((address_space(3)))
typedef unsigned short u16;
typedef __bf16 bf16x8 __attribute__((ext_vector_type(8)));
typedef __bf16 bf16x2 __attribute__((ext_vector_type(2)));
typedef short s16x4 __attribute__((ext_vector_type(4)));
typedef short s16x8 __attribute__((ext_vector_type(8)));
typedef float f32x2 __attribute__((ext_vector_type(2)));
typedef float f32x4 __attribute__((ext_vector_type(4)));
typedef float f32x16 __attribute__((ext_vector_type(16)));
typedef unsigned u32x4 __attribute__((ext_vector_type(4)));
typedef unsigned u32x2 __attribute__((ext_vector_type(2)));
typedef __amdgpu_buffer_rsrc_t rsrc_t;
typedef LAS char* lptr;

constexpr int S = 2048, DM = 640, NH = 32, HD = 128, DC = 512;
constexpr size_t MB = 1u << 20;
constexpr size_t WS_K = 0, WS_V = 16 * MB, WS_CTX = 32 * MB, WS_PART = 48 * MB, WS_UR = 72 * MB, WS_ZT = 76 * MB;

#define SBAR() __builtin_amdgcn_sched_barrier(0)
#define WAIT_V(n) asm volatile("s_waitcnt vmcnt(" #n ")" ::: "memory")
#define WAIT_L(n) asm volatile("s_waitcnt lgkmcnt(" #n ")" ::: "memory")
#define BAR() asm volatile("s_barrier" ::: "memory")
#define MFMA32(a, b, c) __builtin_amdgcn_mfma_f32_32x32x16_bf16((a), (b), (c), 0, 0, 0)
#define MFMA16(a, b, c) __builtin_amdgcn_mfma_f32_16x16x32_bf16((a), (b), (c), 0, 0, 0)

DI unsigned rfl(unsigned x) { return __builtin_amdgcn_readfirstlane(x); }
DI unsigned cvtpk(float lo, float hi) { f32x2 v = {lo, hi}; bf16x2 b = __builtin_convertvector(v, bf16x2); return __builtin_bit_cast(unsigned, b); }
DI float bf2f(unsigned b16) { return __uint_as_float(b16 << 16); }
DI float rbf(float x) { return (float)(__bf16)x; }
DI rsrc_t mkrsrc(const void* p, unsigned bytes) { return __builtin_amdgcn_make_buffer_rsrc((void*)p, (short)0, (int)bytes, 0x00020000); }
DI void dma16(rsrc_t r, unsigned voff, unsigned soff, unsigned lds) {
    asm volatile("s_mov_b32 m0, %0\n\ts_nop 0\n\tbuffer_load_dwordx4 %1, %2, %3 offen lds" ::"s"(lds), "v"(voff), "s"(r), "s"(soff) : "memory");
}
DI void dma16x4(rsrc_t r, unsigned voff, unsigned soff, unsigned lds) {
    asm volatile("s_mov_b32 m0, %0\n\ts_nop 0\n\tbuffer_load_dwordx4 %1, %2, %3 offen lds\n\tbuffer_load_dwordx4 %1, %2, %3 offen offset:1024 lds\n\t"
                 "buffer_load_dwordx4 %1, %2, %3 offen offset:2048 lds\n\tbuffer_load_dwordx4 %1, %2, %3 offen offset:3072 lds" ::"s"(lds), "v"(voff), "s"(r), "s"(soff) : "memory");
}
DI bf16x8 ldsfrag(lptr p) { return *(LAS bf16x8*)p; }
typedef short v4i16_t __attribute__((ext_vector_type(4)));
DI s16x4 trread(lptr p) { return __builtin_amdgcn_ds_read_tr16_b64_v4i16((LAS v4i16_t*)p); }
DI bf16x8 cat8(s16x4 lo, s16x4 hi) { return __builtin_bit_cast(bf16x8, __builtin_shufflevector(lo, hi, 0, 1, 2, 3, 4, 5, 6, 7)); }

DI int lds_byte(int r, int c) { const int st = (r >> 4) * 2 + (c >> 5), rr = r & 15, cc = c & 31, ob = rr * 64 + cc * 2; return st * 1024 + (ob ^ (((ob >> 9) & 1) << 5)); }
DI void stage_rc(int b, int& R, int& C) { const int st = b / 1024, sb = b % 1024, swz = sb ^ (((sb >> 9) & 1) << 5); R = (st >> 1) * 16 + swz / 64; C = (st & 1) * 32 + (swz % 64) / 2; }

constexpr int K1_LDS = 8 * 16384;
__global__ __launch_bounds__(512, 2) void k1_kvproj(const u16* __restrict__ ckv, const u16* __restrict__ wuk, const u16* __restrict__ wuv, char* __restrict__ Kout, char* __restrict__ Vout) {
    extern __shared__ __attribute__((aligned(16))) char smem[];
    const lptr lds = (lptr)smem;
    const unsigned ldsb = (unsigned)(uintptr_t)smem;
    const int tid = threadIdx.x, lane = tid & 63, fr = lane & 15, fq = lane >> 4;
    const int wid = rfl(tid >> 6), wr = wid >> 2, wc = wid & 3;
    const int b = blockIdx.x, head = 4 * (b & 7) + (b >> 6), pm = (b >> 3) & 7;
    const rsrc_t rA = mkrsrc(ckv, S * DC * 2), rK = mkrsrc(wuk, DC * 4096 * 2), rV = mkrsrc(wuv, DC * 4096 * 2);
    unsigned voffA, voffB;
    { int R, C; stage_rc(tid * 16, R, C); voffA = (unsigned)(R * DC + C) * 2u;
      const int row = 4 * wid + (lane >> 4), swz = ((row & 3) << 2) | (((row >> 2) & 1) << 1) | ((row >> 3) & 1);
      voffB = (unsigned)row * 8192u + (unsigned)(((lane & 15) ^ swz) * 16); }
    const unsigned sA0 = (unsigned)pm * 256u * 1024u, sB0 = (unsigned)head * 256u, ldw = ldsb + (unsigned)wid * 1024u;
#define SA(b_, h_) (((b_) * 2 + (h_)) * 16384)
#define SB(b_, h_) ((4 + (b_) * 2 + (h_)) * 16384)
#define STAGE_A(b_, h_, kt) do { dma16(rA, voffA, sA0 + (h_) * 131072u + (unsigned)(kt) * 128u, ldw + SA(b_, h_)); dma16(rA, voffA, sA0 + (h_) * 131072u + (unsigned)(kt) * 128u + 65536u, ldw + SA(b_, h_) + 8192); } while (0)
#define STAGE_B(b_, h_, kt) do { dma16((h_) ? rV : rK, voffB, sB0 + (unsigned)(kt) * 524288u, ldw + SB(b_, h_)); dma16((h_) ? rV : rK, voffB, sB0 + (unsigned)(kt) * 524288u + 262144u, ldw + SB(b_, h_) + 8192); } while (0)
    const int aoff = lds_byte(wr * 64 + fr, fq * 8);
    int boff[2][2];
    { const int g = lane >> 4, q = (lane >> 2) & 3, p = lane & 3;
      const int base = (8 * g + q) * 256 + ((wc ^ q) << 6) + ((p >> 1) << 5) + ((g & 1) << 4) + 8 * (p & 1);
#pragma unroll
      for (int n = 0; n < 2; ++n)
#pragma unroll
          for (int t = 0; t < 2; ++t) boff[n][t] = (base ^ (t << 5) ^ (n << 4)) + t * 1024; }
#define LDA(dst, b_, h_) do { _Pragma("unroll") for (int m = 0; m < 4; ++m) _Pragma("unroll") for (int k = 0; k < 2; ++k) dst[m][k] = ldsfrag(lds + SA(b_, h_) + aoff + m * 2048 + k * 1024); } while (0)
#define LDB(dst, b_, h_) do { _Pragma("unroll") for (int n = 0; n < 2; ++n) _Pragma("unroll") for (int k = 0; k < 2; ++k) \
        dst[n][k] = cat8(trread(lds + SB(b_, h_) + boff[n][0] + k * 8192), trread(lds + SB(b_, h_) + boff[n][1] + k * 8192)); } while (0)
#define MMA(ai, bj, At_, Bt_) do { __builtin_amdgcn_s_setprio(1); _Pragma("unroll") for (int m = 0; m < 4; ++m) _Pragma("unroll") for (int n = 0; n < 2; ++n) _Pragma("unroll") for (int k = 0; k < 2; ++k) \
        acc[ai][bj][m][n] = MFMA16(Bt_[n][k], At_[m][k], acc[ai][bj][m][n]); __builtin_amdgcn_s_setprio(0); } while (0)
    f32x4 acc[2][2][4][2] = {};
    bf16x8 At[4][2], B0[2][2], B1[2][2];
    constexpr int nt = DC / 64;
    STAGE_B(0, 0, 0); STAGE_A(0, 0, 0); STAGE_B(0, 1, 0); STAGE_A(0, 1, 0);
    if (wr == 1) BAR();
    WAIT_V(4); BAR();
    STAGE_B(1, 0, 1); STAGE_A(1, 0, 1); STAGE_B(1, 1, 1);
    WAIT_V(6); BAR();
    for (int t = 0; t < nt - 2; t += 2) {
        LDB(B0, 0, 0); SBAR(); LDA(At, 0, 0); STAGE_A(1, 1, t + 1);
        WAIT_L(8); BAR(); WAIT_L(0); MMA(0, 0, At, B0); BAR(); SBAR();
        LDB(B1, 0, 1); STAGE_B(0, 0, t + 2);
        BAR(); WAIT_L(0); MMA(0, 1, At, B1); BAR();
        LDA(At, 0, 1); STAGE_A(0, 0, t + 2);
        BAR(); WAIT_L(0); MMA(1, 0, At, B0); BAR(); SBAR();
        STAGE_B(0, 1, t + 2);
        WAIT_V(6); BAR(); MMA(1, 1, At, B1); BAR();
        LDB(B0, 1, 0); SBAR(); LDA(At, 1, 0); STAGE_A(0, 1, t + 2);
        WAIT_L(8); BAR(); WAIT_L(0); MMA(0, 0, At, B0); BAR(); SBAR();
        LDB(B1, 1, 1); STAGE_B(1, 0, t + 3);
        BAR(); WAIT_L(0); MMA(0, 1, At, B1); BAR();
        LDA(At, 1, 1); STAGE_A(1, 0, t + 3);
        BAR(); WAIT_L(0); MMA(1, 0, At, B0); BAR(); SBAR();
        STAGE_B(1, 1, t + 3);
        WAIT_V(6); BAR(); MMA(1, 1, At, B1); BAR();
    }
    { LDB(B0, 0, 0); WAIT_V(0); LDA(At, 0, 0); STAGE_A(1, 1, nt - 1);
      BAR(); WAIT_L(0); MMA(0, 0, At, B0); BAR();
      LDB(B1, 0, 1); BAR(); WAIT_L(0); MMA(0, 1, At, B1); BAR();
      LDA(At, 0, 1); WAIT_V(4); BAR(); WAIT_L(0); MMA(1, 0, At, B0); MMA(1, 1, At, B1); BAR(); }
    { LDB(B0, 1, 0); LDA(At, 1, 0); WAIT_V(2); BAR(); WAIT_L(0); MMA(0, 0, At, B0); BAR();
      LDB(B1, 1, 1); WAIT_V(0); BAR(); WAIT_L(0); MMA(0, 1, At, B1); BAR();
      LDA(At, 1, 1); BAR(); WAIT_L(0); MMA(1, 0, At, B0); MMA(1, 1, At, B1); BAR(); }
    if (wr == 0) BAR();
    char* Kh = Kout + (size_t)head * 524288; char* Vh = Vout + (size_t)head * 524288;
#pragma unroll
    for (int ai = 0; ai < 2; ++ai) {
        const int tile = 4 * pm + 2 * ai + wr;
#pragma unroll
        for (int m = 0; m < 4; ++m) {
            const int kk = 16 * m + fr;
#pragma unroll
            for (int bj = 0; bj < 2; ++bj) {
                const f32x4 v0 = acc[ai][bj][m][0], v1 = acc[ai][bj][m][1];
                u32x4 w; w.x = cvtpk(v0[0], v0[1]); w.y = cvtpk(v0[2], v0[3]); w.z = cvtpk(v1[0], v1[1]); w.w = cvtpk(v1[2], v1[3]);
                if (bj == 0) *(u32x4*)(Kh + tile * 16384 + (4 * wc + fq) * 1024 + kk * 16) = w;
                else *(u32x4*)(Vh + tile * 16384 + ((kk >> 3) * 4 + wc) * 512 + (kk & 7) * 64 + fq * 16) = w;
            }
        }
    }
#undef SA
#undef SB
#undef STAGE_A
#undef STAGE_B
#undef LDA
#undef LDB
#undef MMA
}

constexpr int K2_LDS = 163840;
constexpr float QSCALE = 0.08838834764831845f * 1.4426950408889634f;
constexpr float THR = 8.0f;
constexpr int L_K = 0, L_V = 65536, L_XB = 98304, L_PARK = 131072;

DI float max3f(float a, float b, float c) { return __builtin_fmaxf(__builtin_fmaxf(a, b), c); }

__global__ __launch_bounds__(512, 2) void k2_attn(const u16* __restrict__ x, const u16* __restrict__ wq, const char* __restrict__ Kg, const char* __restrict__ Vg, u16* __restrict__ ctx) {
    extern __shared__ __attribute__((aligned(16))) char smem[];
    const lptr lds = (lptr)smem;
    const unsigned ldsb = (unsigned)(uintptr_t)smem;
    const int tid = threadIdx.x, lane = tid & 63, r32 = lane & 31, hi = lane >> 5;
    const int wid = rfl(tid >> 6), grp = wid >> 2, wq4 = wid & 3;
    const int b = blockIdx.x, head = 4 * (b & 7) + (b >> 6), xi = (b >> 3) & 7;
    const int blkB = 15 - xi, blkA = xi;
    const rsrc_t rK = mkrsrc(Kg + (size_t)head * 524288, 524288), rV = mkrsrc(Vg + (size_t)head * 524288, 524288);
    const unsigned vlin = (unsigned)lane * 16u;
    const unsigned kdst = ldsb + L_K + grp * 32768 + wq4 * 4096, vdst = ldsb + L_V + grp * 16384 + wq4 * 4096;
#define DMA_K(T, slot) dma16x4(rK, vlin, (unsigned)(T) * 16384u + wq4 * 4096u, kdst + (slot) * 16384u)
#define DMA_V(T, slot) dma16x4(rV, vlin, (unsigned)(T) * 16384u + wq4 * 4096u, vdst + (slot) * 32768u)
    bf16x8 q[8];
    {
        const rsrc_t rX = mkrsrc(x, S * DM * 2), rW = mkrsrc(wq, DM * 4096 * 2);
        const unsigned grow = (unsigned)((grp ? blkA : blkB) * 128 + wq4 * 32);
        unsigned vx[2];
#pragma unroll
        for (int j = 0; j < 2; ++j) vx[j] = (unsigned)(lane >> 3) * 1280u + (unsigned)(((lane & 7) ^ (4 * j + (lane >> 4))) * 16);
        const unsigned vw = (unsigned)((lane >> 2) & 7) * 8192u + (unsigned)(lane >> 5) * 64u + (unsigned)(lane & 3) * 16u;
        const unsigned sx0 = grow * 1280u, sw0 = (unsigned)wid * 8u * 8192u + (unsigned)head * 256u;
#define QISSUE(t, st) do { const unsigned sb_ = ldsb + (st) * 49152u; \
            _Pragma("unroll") for (int j = 0; j < 4; ++j) dma16(rX, vx[j & 1], sx0 + j * 8u * 1280u + (unsigned)(t) * 128u, sb_ + wid * 4096u + j * 1024u); \
            _Pragma("unroll") for (int p = 0; p < 2; ++p) dma16(rW, vw, sw0 + (unsigned)(t) * 64u * 8192u + p * 128u, sb_ + 32768u + (wid * 2 + p) * 1024u); } while (0)
        const int row = wid * 32 + r32;
        const int xb = row * 128 + ((hi ^ ((row >> 1) & 7)) << 4);
        const int wb = 32768 + 8 * (lane & 3) + 64 * ((lane >> 2) & 3) + 32 * ((lane >> 4) & 1) + 2048 * hi;
        f32x16 qa[4] = {};
        QISSUE(0, 2); QISSUE(1, 0);
        int st = 2;
        for (int t = 0; t < 10; ++t) {
            if (t < 9) WAIT_V(6); else WAIT_V(0);
            BAR();
            const int st2 = st == 0 ? 2 : st - 1;
            if (t < 8) QISSUE(t + 2, st2);
            else if (t == 9) { DMA_K(grp, 0); DMA_V(grp, 0); if (blkB > 0) DMA_K(2 + grp, 1); }
            const lptr sb = lds + st * 49152;
#pragma unroll
            for (int ks = 0; ks < 4; ++ks) {
                const bf16x8 xf = ldsfrag(sb + (xb ^ (ks << 5)));
#pragma unroll
                for (int db = 0; db < 4; ++db) {
                    const bf16x8 wf = cat8(trread(sb + wb + 4096 * ks + 512 * db), trread(sb + wb + 4096 * ks + 512 * db + 256));
                    qa[db] = MFMA32(wf, xf, qa[db]);
                }
            }
            st = st == 2 ? 0 : st + 1;
        }
#undef QISSUE
#pragma unroll
        for (int s = 0; s < 8; ++s) {
            u32x4 w;
#pragma unroll
            for (int e = 0; e < 4; ++e) w[e] = cvtpk(qa[s >> 1][8 * (s & 1) + 2 * e] * QSCALE, qa[s >> 1][8 * (s & 1) + 2 * e + 1] * QSCALE);
            q[s] = __builtin_bit_cast(bf16x8, w);
        }
        WAIT_L(0); BAR();
        { const lptr dst = lds + (grp ? L_PARK : L_XB) + wq4 * 8192 + lane * 16;
#pragma unroll
          for (int s = 0; s < 8; ++s) *(LAS bf16x8*)(dst + s * 1024) = q[s]; }
        WAIT_L(0); BAR();
        if (grp) {
#pragma unroll
            for (int s = 0; s < 8; ++s) q[s] = ldsfrag(lds + L_XB + wq4 * 8192 + lane * 16 + s * 1024);
            WAIT_L(0);
        }
    }
    const int kb0 = L_K + grp * 32768 + hi * 1024 + r32 * 16;
    const int vb0 = L_V + grp * 16384 + 8 * (lane & 3) + 64 * ((lane >> 2) & 3) + 32 * ((lane >> 4) & 1) + 256 * hi;
    for (int bi = 0; bi < 2; ++bi) {
        const int blk = bi ? blkA : blkB, n = blk + 1;
        if (bi) {
#pragma unroll
            for (int s = 0; s < 8; ++s) q[s] = ldsfrag(lds + L_PARK + wq4 * 8192 + lane * 16 + s * 1024);
            DMA_K(grp, 0); DMA_V(grp, 0); if (n > 1) DMA_K(2 + grp, 1);
        }
        f32x16 o[4] = {};
        f32x16 s0, s1, negm = {};
        float mref = 0.f, lsum = 0.f;
        bf16x8 kf[16], vf[16], pp[4];
        int kb = kb0, vb = vb0;
        WAIT_V(0); BAR();
        if (grp) BAR();
#define KFRAG(i) ldsfrag(lds + kb + ((i) >> 1) * 2048 + ((i) & 1) * 512)
#define VFRAG(j) cat8(trread(lds + vbp + ((j) >> 2) * 4096 + ((j) & 3) * 512), trread(lds + vbp + ((j) >> 2) * 4096 + ((j) & 3) * 512 + 2048))
#define QK(PREV) do { _Pragma("unroll") for (int i = 0; i < 16; ++i) { \
            if (i + 4 < 16) kf[i + 4] = KFRAG(i + 4); else if (PREV) vf[i + 4 - 16] = VFRAG(i + 4 - 16); \
            if (i == 0) s0 = MFMA32(kf[0], q[0], negm); else if (i == 1) s1 = MFMA32(kf[1], q[0], negm); \
            else if (i & 1) s1 = MFMA32(kf[i], q[i >> 1], s1); else s0 = MFMA32(kf[i], q[i >> 1], s0); \
            SBAR(); } } while (0)
#define PV() do { _Pragma("unroll") for (int j = 0; j < 16; ++j) { \
            if (j + 4 < 16) vf[j + 4] = VFRAG(j + 4); \
            o[j & 3] = MFMA32(vf[j], pp[j >> 2], o[j & 3]); \
            SBAR(); } } while (0)
        {
            const int vbp = vb; (void)vbp;
#pragma unroll
            for (int i = 0; i < 4; ++i) kf[i] = KFRAG(i);
            __builtin_amdgcn_s_setprio(1);
            QK(false);
            __builtin_amdgcn_s_setprio(0);
        }
        for (int i = 0; i < n; ++i) {
            WAIT_V(0); BAR();
            if (i + 2 < n) DMA_K(2 * (i + 2) + grp, i & 1);
            if (i + 1 < n) DMA_V(2 * (i + 1) + grp, (i + 1) & 1);
            if (i == n - 1) {
                const int th0 = wq4 * 32 + r32 - 64 * grp - 4 * hi, th1 = th0 - 32;
#pragma unroll
                for (int r = 0; r < 16; ++r) { const int c = (r & 3) + 8 * (r >> 2); if (c > th0) s0[r] = -INFINITY; if (c > th1) s1[r] = -INFINITY; }
            }
            float ma = max3f(s0[0], s0[1], s1[0]), mb = max3f(s0[2], s0[3], s1[1]); ma = max3f(ma, s1[2], s1[3]);
#pragma unroll
            for (int r = 4; r < 16; r += 4) { ma = max3f(ma, s0[r], s0[r + 1]); mb = max3f(mb, s0[r + 2], s0[r + 3]); ma = max3f(ma, s1[r], s1[r + 1]); mb = max3f(mb, s1[r + 2], s1[r + 3]); }
            float rm = __builtin_fmaxf(ma, mb);
            { auto rr = __builtin_amdgcn_permlane32_swap(__float_as_uint(rm), __float_as_uint(rm), false, false); rm = __builtin_fmaxf(__uint_as_float(rr[0]), __uint_as_float(rr[1])); }
            if (__builtin_expect(i == 0 || __any(rm > THR), 0)) {
                const float dl = __builtin_fmaxf(rm, i == 0 ? -1e30f : 0.f), f = i == 0 ? 0.f : __builtin_amdgcn_exp2f(-dl);
                mref += dl; lsum *= f;
#pragma unroll
                for (int r = 0; r < 16; ++r) { s0[r] -= dl; s1[r] -= dl; negm[r] = -mref; }
#pragma unroll
                for (int d = 0; d < 4; ++d)
#pragma unroll
                    for (int r = 0; r < 16; ++r) o[d][r] *= f;
            }
            float la = 0.f, lb = 0.f;
#pragma unroll
            for (int r = 0; r < 16; ++r) { s0[r] = __builtin_amdgcn_exp2f(s0[r]); s1[r] = __builtin_amdgcn_exp2f(s1[r]); }
#pragma unroll
            for (int r = 0; r < 16; ++r) { la += s0[r]; lb += s1[r]; }
            lsum += la + lb;
#pragma unroll
            for (int ks = 0; ks < 4; ++ks) {
                u32x4 w;
#pragma unroll
                for (int e = 0; e < 4; ++e) w[e] = (ks >> 1) ? cvtpk(s1[8 * (ks & 1) + 2 * e], s1[8 * (ks & 1) + 2 * e + 1]) : cvtpk(s0[8 * (ks & 1) + 2 * e], s0[8 * (ks & 1) + 2 * e + 1]);
                pp[ks] = __builtin_bit_cast(bf16x8, w);
            }
            const int vbp = vb;
            kb ^= 16384; vb ^= 32768;
            const bool more = i + 1 < n;
            if (more) {
#pragma unroll
                for (int u = 0; u < 4; ++u) kf[u] = KFRAG(u);
            } else {
#pragma unroll
                for (int u = 0; u < 4; ++u) vf[u] = VFRAG(u);
            }
            BAR();
            __builtin_amdgcn_s_setprio(1);
            if (more) QK(true);
            PV();
            __builtin_amdgcn_s_setprio(0);
        }
        if (!grp) BAR();
        { auto rr = __builtin_amdgcn_permlane32_swap(__float_as_uint(lsum), __float_as_uint(lsum), false, false); lsum = __uint_as_float(rr[0]) + __uint_as_float(rr[1]); }
        BAR();
        LAS f32x2* ml = (LAS f32x2*)(lds + 65536);
        if (hi == 0) ml[wid * 32 + r32] = (f32x2){mref, lsum};
        WAIT_L(0); BAR();
        const f32x2 oth = ml[(wid ^ 4) * 32 + r32];
        const float mm = __builtin_fmaxf(mref, oth.x), w0 = __builtin_amdgcn_exp2f(mref - mm), w1 = __builtin_amdgcn_exp2f(oth.x - mm);
        const float sc = w0 * __builtin_amdgcn_rcpf(lsum * w0 + oth.y * w1);
#define MERGE(KEEP, SEND) do { \
        { const lptr dst = lds + wid * 8192 + lane * 16; \
          _Pragma("unroll") for (int e = 0; e < 8; ++e) { const int d = SEND + (e >> 2), r = (e & 3) * 4; \
              *(LAS f32x4*)(dst + e * 1024) = (f32x4){o[d][r] * sc, o[d][r + 1] * sc, o[d][r + 2] * sc, o[d][r + 3] * sc}; } } \
        WAIT_L(0); BAR(); \
        { const lptr src = lds + (wid ^ 4) * 8192 + lane * 16; \
          u16* crow = ctx + (size_t)(blk * 128 + wq4 * 32 + r32) * 4096 + head * 128 + KEEP * 32 + hi * 8; \
          _Pragma("unroll") for (int c = 0; c < 4; ++c) {     \
              const int d = KEEP + (c >> 1), r = (c & 1) * 8; \
              const f32x4 a0 = *(LAS f32x4*)(src + (2 * c) * 1024), a1 = *(LAS f32x4*)(src + (2 * c + 1) * 1024); \
              u32x4 w; w.x = cvtpk(o[d][r] * sc + a0[0], o[d][r + 1] * sc + a0[1]); w.y = cvtpk(o[d][r + 2] * sc + a0[2], o[d][r + 3] * sc + a0[3]); \
              w.z = cvtpk(o[d][r + 4] * sc + a1[0], o[d][r + 5] * sc + a1[1]); w.w = cvtpk(o[d][r + 6] * sc + a1[2], o[d][r + 7] * sc + a1[3]); \
              *(u32x4*)(crow + (c >> 1) * 32 + (c & 1) * 16) = w; } } } while (0)
        if (grp) MERGE(2, 0); else MERGE(0, 2);
#undef MERGE
        WAIT_L(0); BAR();
#undef KFRAG
#undef VFRAG
#undef QK
#undef PV
    }
#undef DMA_K
#undef DMA_V
}

constexpr int K3_STAGE = 73728, K3_LDS = 2 * K3_STAGE;
__global__ __launch_bounds__(512, 2) void k3_oproj(const u16* __restrict__ ctx, const u16* __restrict__ wo, float* __restrict__ part) {
    extern __shared__ __attribute__((aligned(16))) char smem[];
    const lptr lds = (lptr)smem;
    const unsigned ldsb = (unsigned)(uintptr_t)smem;
    const int tid = threadIdx.x, lane = tid & 63, fr = lane & 15, fq = lane >> 4;
    const int wid = rfl(tid >> 6), kg = wid >> 2, wm = (wid >> 1) & 1, wn = wid & 1;
    const int b = blockIdx.x;
    const int pm = (b & 7) * 2 + ((b >> 3) & 1), pn = (b >> 4) & 3, ksp = b >> 6;
    const rsrc_t rA = mkrsrc(ctx, S * 4096 * 2), rB = mkrsrc(wo, 4096 * DM * 2);
    unsigned voffA;
    { int R, C; stage_rc(lane * 16, R, C); voffA = (unsigned)R * 8192u + (unsigned)C * 2u; }
    unsigned voffB[5];
#pragma unroll
    for (int j = 0; j < 5; ++j) { const int pc = (wid * 5 + j) % 20, idx = pc * 64 + lane, row = idx / 20, pos = idx % 20, ch = pos ^ (((row >> 3) & 1) << 1);
        voffB[j] = (unsigned)row * 1280u + (unsigned)ch * 16u; }
    const unsigned sA0 = (unsigned)pm * 128u * 8192u + (unsigned)ksp * 2048u + (unsigned)kg * 128u, sB0 = ((unsigned)ksp * 1024u + (unsigned)kg * 64u) * 1280u + (unsigned)pn * 320u;
#define ISSUE(t, buf) do { const unsigned sb_ = ldsb + (buf) * K3_STAGE; \
        _Pragma("unroll") for (int j = 0; j < 4; ++j) { const int p = (wid & 3) * 4 + j; dma16(rA, voffA, sA0 + (unsigned)(t) * 256u + (unsigned)(p >> 1) * 16u * 8192u + (p & 1) * 64u, sb_ + kg * 16384 + p * 1024); } \
        _Pragma("unroll") for (int j = 0; j < 5; ++j) dma16(rB, voffB[j], sB0 + (unsigned)(t) * 128u * 1280u, sb_ + 32768 + (wid * 5 + j) * 1024); } while (0)
    const int aoff = kg * 16384 + lds_byte(wm * 64 + fr, fq * 8);
    int bo[2];
    { const int g = lane >> 4, q = (lane >> 2) & 3, p = lane & 3;
      const int base = 32768 + kg * 20480 + (8 * g + q) * 320 + (10 * wn + (p >> 1)) * 16 + 8 * (p & 1);
      const int d = (g & 1) ? 32 : 0;
      bo[0] = base + (wn ? -d : d); bo[1] = base + (wn ? d : -d); }
    f32x4 acc[4][5] = {};
    ISSUE(0, 0);
    for (int t = 0; t < 8; ++t) {
        WAIT_V(0); BAR();
        if (t + 1 < 8) ISSUE(t + 1, (t + 1) & 1);
        const lptr sb = lds + (t & 1) * K3_STAGE;
#pragma unroll
        for (int ks = 0; ks < 2; ++ks) {
            bf16x8 af[4], bfr[5];
#pragma unroll
            for (int m = 0; m < 4; ++m) af[m] = ldsfrag(sb + aoff + m * 2048 + ks * 1024);
#pragma unroll
            for (int nb = 0; nb < 5; ++nb) bfr[nb] = cat8(trread(sb + bo[nb & 1] + nb * 32 + ks * 10240), trread(sb + bo[nb & 1] + nb * 32 + ks * 10240 + 1280));
#pragma unroll
            for (int m = 0; m < 4; ++m)
#pragma unroll
                for (int nb = 0; nb < 5; ++nb) acc[m][nb] = MFMA16(bfr[nb], af[m], acc[m][nb]);
        }
    }
    BAR();
#define RED(KEEP, SEND) do { \
    { const lptr dst = lds + wid * 10240 + lane * 16; \
      _Pragma("unroll") for (int e = 0; e < 10; ++e) *(LAS f32x4*)(dst + e * 1024) = acc[SEND + e / 5][e % 5]; } \
    WAIT_L(0); BAR(); \
    { const lptr src = lds + (wid ^ 4) * 10240 + lane * 16; \
      float* P = part + (size_t)ksp * S * DM + (size_t)(pm * 128 + wm * 64 + fr) * DM + pn * 160 + wn * 80 + fq * 4; \
      _Pragma("unroll") for (int e = 0; e < 10; ++e) { const int m = KEEP + e / 5, nb = e % 5; \
          *(f32x4*)(P + (size_t)m * 16 * DM + nb * 16) = acc[m][nb] + *(LAS f32x4*)(src + e * 1024); } } } while (0)
    if (kg) RED(2, 0); else RED(0, 2);
#undef RED
#undef ISSUE
}

__global__ __launch_bounds__(320) void a1_kv(const u16* __restrict__ k, const u16* __restrict__ v, const u16* __restrict__ shk, u16* __restrict__ ur) {
    __shared__ __attribute__((aligned(16))) char T[40 * 272];
    const int tid = threadIdx.x, t0 = (blockIdx.x >> 4) * 128, d0 = (blockIdx.x & 15) * 40;
    const int c5 = tid % 5, tp = tid / 5, t = t0 + 2 * tp, d = d0 + 8 * c5;
    u32x4 kr[5], vr[2];
#pragma unroll
    for (int j = 0; j < 5; ++j) { const int tt = t - 3 + j; kr[j] = tt >= 0 ? *(const u32x4*)(k + (size_t)tt * DM + d) : (u32x4){0, 0, 0, 0}; }
#pragma unroll
    for (int j = 0; j < 2; ++j) vr[j] = *(const u32x4*)(v + (size_t)(t + j) * DM + d);
#pragma unroll
    for (int e = 0; e < 8; ++e) {
        const u32x2 sw = *(const u32x2*)(shk + (size_t)(d + e) * 4);
        const float w[4] = {bf2f(sw.x & 0xffff), bf2f(sw.x >> 16), bf2f(sw.y & 0xffff), bf2f(sw.y >> 16)};
        float kk[5];
#pragma unroll
        for (int j = 0; j < 5; ++j) kk[j] = bf2f((kr[j][e >> 1] >> (16 * (e & 1))) & 0xffff);
        float o[2];
#pragma unroll
        for (int j = 0; j < 2; ++j) {
            float s = rbf(kk[j] * w[3]);
            s = rbf(s + rbf(kk[j + 1] * w[2])); s = rbf(s + rbf(kk[j + 2] * w[1])); s = rbf(s + rbf(kk[j + 3] * w[0]));
            o[j] = s * bf2f((vr[j][e >> 1] >> (16 * (e & 1))) & 0xffff);
        }
        *(unsigned*)(T + (8 * c5 + e) * 272 + (126 - 2 * tp) * 2) = cvtpk(o[1], o[0]);
    }
    __syncthreads();
    for (int i = tid; i < 640; i += 320) { const int row = i >> 4, ch = i & 15;
        *(u32x4*)(ur + (size_t)(d0 + row) * S + (1920 - t0) + ch * 8) = *(const u32x4*)(T + row * 272 + ch * 16); }
}

constexpr int A2_HC = 4160, A2_U = 8 * A2_HC, A2_UH = 79 * 32, A2_LDS = A2_U + 2 * A2_UH;
__global__ __launch_bounds__(64) void a2_conv(const u16* __restrict__ ur, const u16* __restrict__ ssmk, const u16* __restrict__ ssmD, u16* __restrict__ zT) {
    __shared__ __attribute__((aligned(16))) char sm[A2_LDS];
    const lptr lds = (lptr)sm;
    const int lane = threadIdx.x, d = blockIdx.x;
    const u16* h = ssmk + (size_t)d * S;
#pragma unroll
    for (int it = 0; it < 5; ++it) {
        const int c = it * 64 + lane;
        if (c < 260) {
            const int n0 = 8 * c - 32;
            u32x4 lo = {0, 0, 0, 0}, hi = {0, 0, 0, 0};
            if (n0 >= 0 && n0 < S) lo = *(const u32x4*)(h + n0);
            if (n0 + 8 >= 0 && n0 + 8 < S) hi = *(const u32x4*)(h + n0 + 8);
            const unsigned r[8] = {lo.x, lo.y, lo.z, lo.w, hi.x, hi.y, hi.z, hi.w};
            unsigned sh[7];
#pragma unroll
            for (int i = 0; i < 7; ++i) sh[i] = __builtin_amdgcn_alignbyte(r[i + 1], r[i], 2);
#pragma unroll
            for (int m = 1; m <= 8; ++m) {
                u32x4 w;
                if (m & 1) w = (u32x4){sh[m >> 1], sh[(m >> 1) + 1], sh[(m >> 1) + 2], sh[(m >> 1) + 3]};
                else w = (u32x4){r[m >> 1], r[(m >> 1) + 1], r[(m >> 1) + 2], r[(m >> 1) + 3]};
                *(LAS u32x4*)(lds + (m - 1) * A2_HC + c * 16) = w;
            }
        }
    }
    if (lane < 60) *(LAS u32x4*)(lds + A2_U + (lane / 30) * A2_UH + (lane % 30) * 16) = (u32x4){0, 0, 0, 0};
#pragma unroll
    for (int it = 0; it < 4; ++it) {
        const int c = it * 64 + lane, J = 63 - (c >> 2), g = c & 3;
        *(LAS u32x4*)(lds + A2_U + (g >> 1) * A2_UH + (J + 15) * 32 + (g & 1) * 16) = *(const u32x4*)(ur + (size_t)d * S + c * 8);
    }
    __syncthreads();
    const int i = lane & 15, g = lane >> 4;
    const int ab0 = (i & 3) * A2_HC + ((i >> 2) + g) * 16;
    const int bb = A2_U + (g >> 1) * A2_UH + (i + 15) * 32 + (g & 1) * 16;
    f32x4 acc[4][2] = {};
    for (int f = 0; f < 16; ++f) {
        bf16x8 bq[4];
#pragma unroll
        for (int c = 0; c < 4; ++c) bq[c] = ldsfrag(lds + bb + (16 * c - f) * 32);
#pragma unroll
        for (int e = 0; e < 4; ++e) {
            const int dd = 16 * e + f;
            const bf16x8 a0 = ldsfrag(lds + ab0 + dd * 64), a1 = ldsfrag(lds + ab0 + 4 * A2_HC + dd * 64);
#pragma unroll
            for (int c = e; c < 4; ++c) { acc[c][0] = MFMA16(a0, bq[c - e], acc[c][0]); acc[c][1] = MFMA16(a1, bq[c - e], acc[c][1]); }
        }
    }
    const float D = bf2f(ssmD[d]);
#pragma unroll
    for (int c = 0; c < 4; ++c) {
        const int J = 16 * c + i;
        const u32x4 uu = *(LAS u32x4*)(lds + A2_U + ((3 - g) >> 1) * A2_UH + (J + 15) * 32 + ((3 - g) & 1) * 16);
        float z[8];
#pragma unroll
        for (int e = 0; e < 8; ++e) { const int ep = 7 - e; const float u = bf2f((uu[ep >> 1] >> (16 * (ep & 1))) & 0xffff);
            z[e] = rbf(acc[c][e >> 2][e & 3]) + rbf(u * D); }
        u32x4 w; w.x = cvtpk(z[0], z[1]); w.y = cvtpk(z[2], z[3]); w.z = cvtpk(z[4], z[5]); w.w = cvtpk(z[6], z[7]);
        *(u32x4*)(zT + (size_t)d * S + 32 * J + 8 * g) = w;
    }
}

__global__ __launch_bounds__(512) void k4_mix(const u16* __restrict__ x, const u16* __restrict__ gw, const u16* __restrict__ zT, const float* __restrict__ part, u16* __restrict__ out) {
    __shared__ __attribute__((aligned(16))) u16 zs[8][DM + 8];
    const int tid = threadIdx.x, lane = tid & 63, w = tid >> 6, t0 = blockIdx.x * 8, t = t0 + w;
    for (int d = tid; d < DM; d += 512) { const u32x4 zz = *(const u32x4*)(zT + (size_t)d * S + t0);
#pragma unroll
        for (int e = 0; e < 8; ++e) zs[e][d] = (u16)((zz[e >> 1] >> (16 * (e & 1))) & 0xffff); }
    u32x4 xq[2]; f32x4 bs[2][2];
    float gs = 0.f;
#pragma unroll
    for (int p = 0; p < 2; ++p) {
        const int ch = p * 64 + lane;
        if (ch < 80) {
            xq[p] = *(const u32x4*)(x + (size_t)t * DM + ch * 8);
            const u32x4 gg = *(const u32x4*)(gw + ch * 8);
            const float* P = part + (size_t)t * DM + ch * 8;
#pragma unroll
            for (int hh = 0; hh < 2; ++hh) bs[p][hh] = (*(const f32x4*)(P + hh * 4) + *(const f32x4*)(P + (size_t)S * DM + hh * 4)) + (*(const f32x4*)(P + (size_t)2 * S * DM + hh * 4) + *(const f32x4*)(P + (size_t)3 * S * DM + hh * 4));
#pragma unroll
            for (int e = 0; e < 8; ++e) gs += rbf(bf2f((xq[p][e >> 1] >> (16 * (e & 1))) & 0xffff) * bf2f((gg[e >> 1] >> (16 * (e & 1))) & 0xffff));
        }
    }
#pragma unroll
    for (int o = 32; o >= 1; o >>= 1) gs += __shfl_xor(gs, o);
    const float g = rbf(1.f / (1.f + __expf(-rbf(gs)))), g1 = rbf(1.f - g);
    __syncthreads();
#pragma unroll
    for (int p = 0; p < 2; ++p) {
        const int ch = p * 64 + lane;
        if (ch < 80) {
            const u32x4 zz = *(const u32x4*)(&zs[w][ch * 8]);
            float o[8];
#pragma unroll
            for (int e = 0; e < 8; ++e) {
                const float a = rbf(bf2f((xq[p][e >> 1] >> (16 * (e & 1))) & 0xffff) * bf2f((zz[e >> 1] >> (16 * (e & 1))) & 0xffff));
                o[e] = rbf(g * a) + rbf(g1 * rbf(bs[p][e >> 2][e & 3]));
            }
            u32x4 wv; wv.x = cvtpk(o[0], o[1]); wv.y = cvtpk(o[2], o[3]); wv.z = cvtpk(o[4], o[5]); wv.w = cvtpk(o[6], o[7]);
            *(u32x4*)(out + (size_t)t * DM + ch * 8) = wv;
        }
    }
}

extern "C" void kernel_launch(void* const* d_in, const int* in_sizes, int n_in, void* d_out, int out_size, void* d_ws, size_t ws_size, hipStream_t stream) {
    (void)in_sizes; (void)n_in; (void)out_size; (void)ws_size;
    const u16* Aq = (const u16*)d_in[0]; const u16* Ak = (const u16*)d_in[1]; const u16* Av = (const u16*)d_in[2];
    const u16* shk = (const u16*)d_in[3]; const u16* ssmk = (const u16*)d_in[4]; const u16* ssmD = (const u16*)d_in[5];
    const u16* ckv = (const u16*)d_in[7]; const u16* wq = (const u16*)d_in[8]; const u16* wuk = (const u16*)d_in[9];
    const u16* wuv = (const u16*)d_in[10]; const u16* wo = (const u16*)d_in[11]; const u16* gw = (const u16*)d_in[12];
    char* ws = (char*)d_ws;
    (void)hipFuncSetAttribute((const void*)k1_kvproj, hipFuncAttributeMaxDynamicSharedMemorySize, K1_LDS);
    (void)hipFuncSetAttribute((const void*)k2_attn, hipFuncAttributeMaxDynamicSharedMemorySize, K2_LDS);
    (void)hipFuncSetAttribute((const void*)k3_oproj, hipFuncAttributeMaxDynamicSharedMemorySize, K3_LDS);
    a1_kv<<<256, 320, 0, stream>>>(Ak, Av, shk, (u16*)(ws + WS_UR));
    a2_conv<<<640, 64, 0, stream>>>((const u16*)(ws + WS_UR), ssmk, ssmD, (u16*)(ws + WS_ZT));
    k1_kvproj<<<256, 512, K1_LDS, stream>>>(ckv, wuk, wuv, ws + WS_K, ws + WS_V);
    k2_attn<<<256, 512, K2_LDS, stream>>>(Aq, wq, ws + WS_K, ws + WS_V, (u16*)(ws + WS_CTX));
    k3_oproj<<<256, 512, K3_LDS, stream>>>((const u16*)(ws + WS_CTX), wo, (float*)(ws + WS_PART));
    k4_mix<<<256, 512, 0, stream>>>(Aq, gw, (const u16*)(ws + WS_ZT), (const float*)(ws + WS_PART), (u16*)d_out);
}
```

```cpp
#include <hip/hip_runtime.h>
#include <stdint.h>

#define DI __device__ __forceinline__
#define LAS __attribute__((address_space(3)))
typedef unsigned short u16;
typedef __bf16 bf16x8 __attribute__((ext_vector_type(8)));
typedef __bf16 bf16x2 __attribute__((ext_vector_type(2)));
typedef short s16x4 __attribute__((ext_vector_type(4)));
typedef short s16x8 __attribute__((ext_vector_type(8)));
typedef float f32x2 __attribute__((ext_vector_type(2)));
typedef float f32x4 __attribute__((ext_vector_type(4)));
typedef float f32x16 __attribute__((ext_vector_type(16)));
typedef unsigned u32x4 __attribute__((ext_vector_type(4)));
typedef unsigned u32x2 __attribute__((ext_vector_type(2)));
typedef __amdgpu_buffer_rsrc_t rsrc_t;
typedef LAS char* lptr;

constexpr int S = 2048, DM = 640, NH = 32, HD = 128, DC = 512;
constexpr size_t MB = 1u << 20;
constexpr size_t WS_K = 0, WS_V = 16 * MB, WS_CTX = 32 * MB, WS_PART = 48 * MB, WS_UR = 72 * MB, WS_ZT = 76 * MB;

#define SBAR() __builtin_amdgcn_sched_barrier(0)
#define WAIT_V(n) asm volatile("s_waitcnt vmcnt(" #n ")" ::: "memory")
#define WAIT_L(n) asm volatile("s_waitcnt lgkmcnt(" #n ")" ::: "memory")
#define BAR() asm volatile("s_barrier" ::: "memory")
#define MFMA32(a, b, c) __builtin_amdgcn_mfma_f32_32x32x16_bf16((a), (b), (c), 0, 0, 0)
#define MFMA16(a, b, c) __builtin_amdgcn_mfma_f32_16x16x32_bf16((a), (b), (c), 0, 0, 0)

DI unsigned rfl(unsigned x) { return __builtin_amdgcn_readfirstlane(x); }
DI unsigned cvtpk(float lo, float hi) { f32x2 v = {lo, hi}; bf16x2 b = __builtin_convertvector(v, bf16x2); return __builtin_bit_cast(unsigned, b); }
DI float bf2f(unsigned b16) { return __uint_as_float(b16 << 16); }
DI float rbf(float x) { return (float)(__bf16)x; }
DI rsrc_t mkrsrc(const void* p, unsigned bytes) { return __builtin_amdgcn_make_buffer_rsrc((void*)p, (short)0, (int)bytes, 0x00020000); }
DI void dma16(rsrc_t r, unsigned voff, unsigned soff, unsigned lds) {
    asm volatile("s_mov_b32 m0, %0\n\ts_nop 0\n\tbuffer_load_dwordx4 %1, %2, %3 offen lds" ::"s"(lds), "v"(voff), "s"(r), "s"(soff) : "memory");
}
DI void dma16x4(rsrc_t r, unsigned voff, unsigned soff, unsigned lds) {
    asm volatile("s_mov_b32 m0, %0\n\ts_nop 0\n\tbuffer_load_dwordx4 %1, %2, %3 offen lds\n\tbuffer_load_dwordx4 %1, %2, %3 offen offset:1024 lds\n\t"
                 "buffer_load_dwordx4 %1, %2, %3 offen offset:2048 lds\n\tbuffer_load_dwordx4 %1, %2, %3 offen offset:3072 lds" ::"s"(lds), "v"(voff), "s"(r), "s"(soff) : "memory");
}
template <int P, int BIT> DI void dma16p(rsrc_t r, unsigned voff, unsigned soff, unsigned lds, unsigned en) {
    asm volatile("s_bitcmp1_b32 %4, %6\n\ts_cbranch_scc0 1f\n\ts_mov_b32 m0, %0\n\ts_nop 0\n\tbuffer_load_dwordx4 %1, %2, %3 offen offset:%5 lds\n1:" ::"s"(lds), "v"(voff), "s"(r), "s"(soff), "s"(en), "i"(P * 1024), "i"(BIT) : "memory", "scc");
}
DI void dma16x2(rsrc_t r, unsigned voff, unsigned soff, unsigned lds) {
    asm volatile("s_mov_b32 m0, %0\n\ts_nop 0\n\tbuffer_load_dwordx4 %1, %2, %3 offen lds\n\tbuffer_load_dwordx4 %1, %2, %3 offen offset:1024 lds" ::"s"(lds), "v"(voff), "s"(r), "s"(soff) : "memory");
}
DI void setm0(unsigned lds) { asm volatile("s_mov_b32 m0, %0\n\ts_nop 0" ::"s"(lds) : "memory"); }
template <int OFF> DI void dma16o(rsrc_t r, unsigned voff, unsigned soff) { asm volatile("buffer_load_dwordx4 %0, %1, %2 offen offset:%3 lds" ::"v"(voff), "s"(r), "s"(soff), "i"(OFF) : "memory"); }
DI bf16x8 ldsfrag(lptr p) { return *(LAS bf16x8*)p; }
typedef short v4i16_t __attribute__((ext_vector_type(4)));
DI s16x4 trread(lptr p) { return __builtin_amdgcn_ds_read_tr16_b64_v4i16((LAS v4i16_t*)p); }
DI bf16x8 cat8(s16x4 lo, s16x4 hi) { return __builtin_bit_cast(bf16x8, __builtin_shufflevector(lo, hi, 0, 1, 2, 3, 4, 5, 6, 7)); }

#define PH(k) do {} while (0)
#define PHM0(idx) do {} while (0)
#define PHINIT() do {} while (0)
#define STAMP_DECL do {} while (0)
#define STAMP(k) do {} while (0)
#define STAMP_OUT() do {} while (0)
#define DBGARG
#define DBGPASS(off)
DI int lds_byte(int r, int c) { const int st = (r >> 4) * 2 + (c >> 5), rr = r & 15, cc = c & 31, ob = rr * 64 + cc * 2; return st * 1024 + (ob ^ (((ob >> 9) & 1) << 5)); }
DI void stage_rc(int b, int& R, int& C) { const int st = b / 1024, sb = b % 1024, swz = sb ^ (((sb >> 9) & 1) << 5); R = (st >> 1) * 16 + swz / 64; C = (st & 1) * 32 + (swz % 64) / 2; }

#define EXP_K1 0
constexpr int K1_LDS = 8 * 16384;
__global__ __launch_bounds__(512, 2) void k1_kvproj(const u16* __restrict__ ckv, const u16* __restrict__ wuk, const u16* __restrict__ wuv, char* __restrict__ Kout, char* __restrict__ Vout DBGARG) {
    STAMP_DECL; STAMP(0);
    extern __shared__ __attribute__((aligned(16))) char smem[];
    const lptr lds = (lptr)smem;
    const unsigned ldsb = (unsigned)(uintptr_t)smem;
    const int tid = threadIdx.x, lane = tid & 63, fr = lane & 15, fq = lane >> 4;
    const int wid = rfl(tid >> 6), wr = wid >> 2, wc = wid & 3;
    const int b = blockIdx.x, head = 4 * (b & 7) + (b >> 6), pm = (b >> 3) & 7;
    const rsrc_t rA = mkrsrc(ckv, S * DC * 2), rK = mkrsrc(wuk, DC * 4096 * 2), rV = mkrsrc(wuv, DC * 4096 * 2);
    unsigned voffA, voffB;
    { int R, C; stage_rc(tid * 16, R, C); voffA = (unsigned)(R * DC + C) * 2u;
      const int row = 4 * wid + (lane >> 4), swz = ((row & 3) << 2) | (((row >> 2) & 1) << 1) | ((row >> 3) & 1);
      voffB = (unsigned)row * 8192u + (unsigned)(((lane & 15) ^ swz) * 16); }
    const unsigned sA0 = (EXP_K1 ? 0u : (unsigned)pm * 256u * 1024u), sB0 = (EXP_K1 ? 0u : (unsigned)head * 256u), ldw = ldsb + (unsigned)wid * 1024u;
#define SA(b_, h_) (((b_) * 2 + (h_)) * 16384)
#define SB(b_, h_) ((4 + (b_) * 2 + (h_)) * 16384)
#define STAGE_A(b_, h_, kt) do { dma16(rA, voffA, sA0 + (h_) * 131072u + (unsigned)(kt) * 128u, ldw + SA(b_, h_)); dma16(rA, voffA, sA0 + (h_) * 131072u + (unsigned)(kt) * 128u + 65536u, ldw + SA(b_, h_) + 8192); } while (0)
#define STAGE_B(b_, h_, kt) do { dma16((h_) ? rV : rK, voffB, sB0 + (unsigned)(kt) * 524288u, ldw + SB(b_, h_)); dma16((h_) ? rV : rK, voffB, sB0 + (unsigned)(kt) * 524288u + 262144u, ldw + SB(b_, h_) + 8192); } while (0)
    const int aoff = lds_byte(wr * 64 + fr, fq * 8);
    int boff[2][2];
    { const int g = lane >> 4, q = (lane >> 2) & 3, p = lane & 3;
      const int base = (8 * g + q) * 256 + ((wc ^ q) << 6) + ((p >> 1) << 5) + ((g & 1) << 4) + 8 * (p & 1);
#pragma unroll
      for (int n = 0; n < 2; ++n)
#pragma unroll
          for (int t = 0; t < 2; ++t) boff[n][t] = (base ^ (t << 5) ^ (n << 4)) + t * 1024; }
#define LDA(dst, b_, h_) do { _Pragma("unroll") for (int m = 0; m < 4; ++m) _Pragma("unroll") for (int k = 0; k < 2; ++k) dst[m][k] = ldsfrag(lds + SA(b_, h_) + aoff + m * 2048 + k * 1024); } while (0)
#define LDB(dst, b_, h_) do { _Pragma("unroll") for (int n = 0; n < 2; ++n) _Pragma("unroll") for (int k = 0; k < 2; ++k) \
        dst[n][k] = cat8(trread(lds + SB(b_, h_) + boff[n][0] + k * 8192), trread(lds + SB(b_, h_) + boff[n][1] + k * 8192)); } while (0)
#define MMA(ai, bj, At_, Bt_) do { __builtin_amdgcn_s_setprio(1); _Pragma("unroll") for (int m = 0; m < 4; ++m) _Pragma("unroll") for (int n = 0; n < 2; ++n) _Pragma("unroll") for (int k = 0; k < 2; ++k) \
        acc[ai][bj][m][n] = MFMA16(Bt_[n][k], At_[m][k], acc[ai][bj][m][n]); __builtin_amdgcn_s_setprio(0); } while (0)
    f32x4 acc[2][2][4][2] = {};
    bf16x8 At[4][2], B0[2][2], B1[2][2];
    constexpr int nt = DC / 64;
    STAGE_B(0, 0, 0); STAGE_A(0, 0, 0); STAGE_B(0, 1, 0); STAGE_A(0, 1, 0);
    if (wr == 1) BAR();
    WAIT_V(4); BAR();
    STAGE_B(1, 0, 1); STAGE_A(1, 0, 1); STAGE_B(1, 1, 1);
    WAIT_V(6); BAR();
    STAMP(1);
    for (int t = 0; t < nt - 2; t += 2) {
        LDB(B0, 0, 0); SBAR(); LDA(At, 0, 0); STAGE_A(1, 1, t + 1);
        WAIT_L(8); BAR(); WAIT_L(0); MMA(0, 0, At, B0); BAR(); SBAR();
        LDB(B1, 0, 1); STAGE_B(0, 0, t + 2);
        BAR(); WAIT_L(0); MMA(0, 1, At, B1); BAR();
        LDA(At, 0, 1); STAGE_A(0, 0, t + 2);
        BAR(); WAIT_L(0); MMA(1, 0, At, B0); BAR(); SBAR();
        STAGE_B(0, 1, t + 2);
        WAIT_V(6); BAR(); MMA(1, 1, At, B1); BAR();
        LDB(B0, 1, 0); SBAR(); LDA(At, 1, 0); STAGE_A(0, 1, t + 2);
        WAIT_L(8); BAR(); WAIT_L(0); MMA(0, 0, At, B0); BAR(); SBAR();
        LDB(B1, 1, 1); STAGE_B(1, 0, t + 3);
        BAR(); WAIT_L(0); MMA(0, 1, At, B1); BAR();
        LDA(At, 1, 1); STAGE_A(1, 0, t + 3);
        BAR(); WAIT_L(0); MMA(1, 0, At, B0); BAR(); SBAR();
        STAGE_B(1, 1, t + 3);
        WAIT_V(6); BAR(); MMA(1, 1, At, B1); BAR();
    }
    { LDB(B0, 0, 0); WAIT_V(0); LDA(At, 0, 0); STAGE_A(1, 1, nt - 1);
      BAR(); WAIT_L(0); MMA(0, 0, At, B0); BAR();
      LDB(B1, 0, 1); BAR(); WAIT_L(0); MMA(0, 1, At, B1); BAR();
      LDA(At, 0, 1); WAIT_V(4); BAR(); WAIT_L(0); MMA(1, 0, At, B0); MMA(1, 1, At, B1); BAR(); }
    { LDB(B0, 1, 0); LDA(At, 1, 0); WAIT_V(2); BAR(); WAIT_L(0); MMA(0, 0, At, B0); BAR();
      LDB(B1, 1, 1); WAIT_V(0); BAR(); WAIT_L(0); MMA(0, 1, At, B1); BAR();
      LDA(At, 1, 1); BAR(); WAIT_L(0); MMA(1, 0, At, B0); MMA(1, 1, At, B1); BAR(); }
    if (wr == 0) BAR();
    STAMP(2);
    char* Kh = Kout + (size_t)head * 524288; char* Vh = Vout + (size_t)head * 524288;
#pragma unroll
    for (int ai = 0; ai < 2; ++ai) {
        const int tile = 4 * pm + 2 * ai + wr;
#pragma unroll
        for (int m = 0; m < 4; ++m) {
            const int kk = 16 * m + fr;
#pragma unroll
            for (int bj = 0; bj < 2; ++bj) {
                const f32x4 v0 = acc[ai][bj][m][0], v1 = acc[ai][bj][m][1];
                u32x4 w; w.x = cvtpk(v0[0], v0[1]); w.y = cvtpk(v0[2], v0[3]); w.z = cvtpk(v1[0], v1[1]); w.w = cvtpk(v1[2], v1[3]);
                if (bj == 0) *(u32x4*)(Kh + tile * 16384 + (4 * wc + fq) * 1024 + kk * 16) = w;
                else *(u32x4*)(Vh + tile * 16384 + ((kk >> 3) * 4 + wc) * 512 + (kk & 7) * 64 + fq * 16) = w;
            }
        }
    }
    STAMP(3); STAMP_OUT();
#undef SA
#undef SB
#undef STAGE_A
#undef STAGE_B
#undef LDA
#undef LDB
#undef MMA
}

constexpr int K2_LDS = 163840;
constexpr float QSCALE = 0.08838834764831845f * 1.4426950408889634f;
constexpr float SUMTHR = 256.0f;
constexpr int L_K = 0, L_V = 65536, L_PARK = 131072;
#define MPRIO 1
#define SPREAD 1
#define QEXP 0
#define PHOFF 0
constexpr int PFD = 5;
DI float max3f(float a, float b, float c) { float r; asm("v_max3_f32 %0, %1, %2, %3" : "=v"(r) : "v"(a), "v"(b), "v"(c)); return r; }
DI float max2f(float a, float b) { float r; asm("v_max_f32_e32 %0, %1, %2" : "=v"(r) : "v"(a), "v"(b)); return r; }
DI float fadd_s(float a, float b) { float r = a + b; asm("" : "+v"(r)); return r; }
DI void mfma_init(f32x16& d, bf16x8 a, bf16x8 b, const f32x16& c) { asm volatile("v_mfma_f32_32x32x16_bf16 %0, %1, %2, %3" : "=&v"(d) : "v"(a), "v"(b), "v"(c)); }
DI void mfma_zero(f32x16& d, bf16x8 a, bf16x8 b) { asm volatile("v_mfma_f32_32x32x16_bf16 %0, %1, %2, 0" : "=&v"(d) : "v"(a), "v"(b)); }
DI void mfma_acc(f32x16& d, bf16x8 a, bf16x8 b) { asm volatile("v_mfma_f32_32x32x16_bf16 %0, %1, %2, %0" : "+v"(d) : "v"(a), "v"(b)); }
DI void bcast16(f32x16& d, float aone, float v) { asm volatile("s_nop 1\n\tv_mfma_f32_32x32x2_f32 %0, %1, %2, 0" : "+v"(d) : "v"(aone), "v"(v)); }
DI void warm_line(unsigned ldsdummy, rsrc_t r, unsigned voff) { asm volatile("s_mov_b32 m0, %0\n\ts_nop 0\n\tbuffer_load_dword %1, %2, 0 offen lds" ::"s"(ldsdummy), "v"(voff), "s"(r) : "memory"); }

constexpr int A2_HC = 4160, A2_U = 4 * A2_HC, A2_UH = 79 * 32, A2_CH = A2_U + 2 * A2_UH;
DI void ld16a(u32x4& d, rsrc_t r, unsigned voff) { asm volatile("buffer_load_dwordx4 %0, %1, %2, 0 offen" : "=v"(d) : "v"(voff), "s"(r) : "memory"); }
typedef unsigned u32x6 __attribute__((ext_vector_type(6)));

__global__ __launch_bounds__(512, 2) void k2_attn(const u16* __restrict__ x, const u16* __restrict__ wq, const char* __restrict__ Kg, const char* __restrict__ Vg, u16* __restrict__ ctx,
                                                  const u16* __restrict__ ur, const u16* __restrict__ ssmk, const u16* __restrict__ ssmD, u16* __restrict__ zT DBGARG) {
    STAMP_DECL; STAMP(0);
    extern __shared__ __attribute__((aligned(16))) char smem[];
    const lptr lds = (lptr)smem;
    const unsigned ldsb = (unsigned)(uintptr_t)smem;
    const int tid = threadIdx.x, lane = tid & 63, r32 = lane & 31, hi = lane >> 5;
    const int wid = rfl(tid >> 6), grp = wid >> 2, wq4 = wid & 3;
    const int b = blockIdx.x, head = 4 * (b & 7) + (b >> 6), xi = (b >> 3) & 7, j32 = b >> 3;
    const int blkB = 15 - xi, blkA = xi, x2 = 2 * xi;
    const rsrc_t rK = mkrsrc(Kg + (size_t)head * 524288, 524288), rV = mkrsrc(Vg + (size_t)head * 524288, 524288);
    const unsigned vlin = (unsigned)lane * 16u;
    bf16x8 q[8];
    {
        const rsrc_t rX = mkrsrc(x, S * DM * 2), rW = mkrsrc(wq, DM * 4096 * 2);
        unsigned vx[2];
#pragma unroll
        for (int j = 0; j < 2; ++j) vx[j] = (unsigned)(lane >> 3) * 1280u + (unsigned)(((lane & 7) ^ (4 * j + (lane >> 4))) * 16);
        const unsigned vw = (unsigned)((lane >> 2) & 7) * 8192u + (unsigned)(lane >> 5) * 64u + (unsigned)(lane & 3) * 16u;
        const unsigned sx0 = (unsigned)((grp ? blkA : blkB) * 128 + wq4 * 32) * 1280u, sw0 = (unsigned)wid * 8u * 8192u + (unsigned)head * 256u;
#define QPIECE(p, t, sb_) do { if ((p) < 4) dma16(rX, vx[(p) & 1], sx0 + (p) * 8u * 1280u + (unsigned)(t) * 128u, (sb_) + wid * 4096u + (p) * 1024u); \
                               else dma16(rW, vw, sw0 + (unsigned)(t) * 64u * 8192u + ((p) & 1) * 128u, (sb_) + 32768u + (wid * 2 + ((p) & 1)) * 1024u); } while (0)
        const unsigned dummy = ldsb + 147456u;
        {
            const int nch = b < 128 ? 3 : 2, ch0 = b < 128 ? 3 * b : 384 + 2 * (b - 128);
            const rsrc_t rH = mkrsrc(ssmk + (size_t)ch0 * S, (unsigned)nch * S * 2), rU = mkrsrc(ur + (size_t)ch0 * S, (unsigned)nch * S * 2);
            u32x4 hlo[2], hhi[2];
#pragma unroll
            for (int rd = 0; rd < 2; ++rd) {
                const int task = tid + 512 * rd, c = task / 260, ck = task - 260 * c, n0 = 8 * ck - 32;
                const bool on = c < nch;
                ld16a(hlo[rd], rH, (on && n0 >= 0) ? (unsigned)(c * S + n0) * 2u : 0x80000000u);
                ld16a(hhi[rd], rH, (on && n0 + 8 >= 0 && n0 + 8 < S) ? (unsigned)(c * S + n0 + 8) * 2u : 0x80000000u);
            }
            if (wid < nch) {
#pragma unroll
                for (int p = 0; p < 4; ++p) { const int J = 32 * (p & 1) + (lane >> 1), g = 2 * (p >> 1) + (lane & 1);
                    dma16(rU, (unsigned)(4 * (63 - J) + g) * 16u, (unsigned)wid * (S * 2u), ldsb + wid * A2_CH + A2_U + (p >> 1) * A2_UH + (15 + 32 * (p & 1)) * 32); }
            }
#pragma unroll
            for (int p = 0; p < 6; ++p) QPIECE(p, 0, ldsb + 2 * 49152u);
            {
                const int i = tid - 192, l = 48 * j32 + i;
                const unsigned ox = (unsigned)(64 * j32 + (tid & 63)) * 1280u + (unsigned)(1 + (tid >> 6)) * 128u;
                const unsigned ow = (unsigned)(64 + (l >> 3)) * 8192u + (unsigned)(b & 7) * 1024u + (unsigned)(l & 7) * 128u;
                if (wid < 3) warm_line(dummy, rX, ox); else warm_line(dummy, rW, tid < 240 ? ow : 0x80000000u);
            }
            if (wid < nch && lane < 60) *(LAS u32x4*)(lds + wid * A2_CH + A2_U + (lane / 30) * A2_UH + (lane % 30) * 16) = (u32x4){0, 0, 0, 0};
            asm volatile("s_waitcnt vmcnt(7)" : "+v"(hlo[0]), "+v"(hhi[0]), "+v"(hlo[1]), "+v"(hhi[1])::"memory");
#pragma unroll
            for (int rd = 0; rd < 2; ++rd) {
                const int task = tid + 512 * rd, c = task / 260, ck = task - 260 * c;
                if (c < nch) {
                    const unsigned r[6] = {hlo[rd].x, hlo[rd].y, hlo[rd].z, hlo[rd].w, hhi[rd].x, hhi[rd].y};
                    unsigned sh[5];
#pragma unroll
                    for (int i = 0; i < 5; ++i) sh[i] = __builtin_amdgcn_alignbyte(r[i + 1], r[i], 2);
                    const lptr dst = lds + c * A2_CH + ck * 16;
                    *(LAS u32x4*)(dst) = (u32x4){sh[0], sh[1], sh[2], sh[3]};
                    *(LAS u32x4*)(dst + A2_HC) = (u32x4){r[1], r[2], r[3], r[4]};
                    *(LAS u32x4*)(dst + 2 * A2_HC) = (u32x4){sh[1], sh[2], sh[3], sh[4]};
                    *(LAS u32x4*)(dst + 3 * A2_HC) = (u32x4){r[2], r[3], r[4], r[5]};
                }
            }
            WAIT_L(0); BAR();
            STAMP(4);
            if (wid < nch) {
                const lptr cb = lds + wid * A2_CH;
                const int i = lane & 15, g = lane >> 4;
                const lptr ab = cb + (i & 3) * A2_HC + ((i >> 2) + g) * 16;
                const lptr bb = cb + A2_U + (g >> 1) * A2_UH + (i + 15) * 32 + (g & 1) * 16;
                f32x4 acc[4][2] = {};
                u32x4 alo[2][4], bq[2][4]; u32x2 aup[2][4];
#define A2LOAD(X, f) do { _Pragma("unroll") for (int c = 0; c < 4; ++c) bq[X][c] = *(LAS u32x4*)(bb + (16 * c - (f)) * 32); \
                    _Pragma("unroll") for (int e = 0; e < 4; ++e) { alo[X][e] = *(LAS u32x4*)(ab + (16 * e + (f)) * 64); aup[X][e] = *(LAS u32x2*)(ab + (16 * e + (f)) * 64 + 16); } SBAR(); } while (0)
#define A2MMA(X) do { _Pragma("unroll") for (int e = 0; e < 4; ++e) { \
                        const u32x6 w6 = {alo[X][e].x, alo[X][e].y, alo[X][e].z, alo[X][e].w, aup[X][e].x, aup[X][e].y}; \
                        const bf16x8 a0 = __builtin_bit_cast(bf16x8, __builtin_shufflevector(w6, w6, 0, 1, 2, 3)), a1 = __builtin_bit_cast(bf16x8, __builtin_shufflevector(w6, w6, 2, 3, 4, 5)); \
                        _Pragma("unroll") for (int c = e; c < 4; ++c) { const bf16x8 bv = __builtin_bit_cast(bf16x8, bq[X][c - e]); acc[c][0] = MFMA16(a0, bv, acc[c][0]); acc[c][1] = MFMA16(a1, bv, acc[c][1]); } } SBAR(); } while (0)
                A2LOAD(0, 0);
                for (int f = 0; f < 16; f += 2) {
                    A2LOAD(1, f + 1);
                    A2MMA(0);
                    A2LOAD(0, (f + 2) & 15);
                    A2MMA(1);
                }
#undef A2LOAD
#undef A2MMA
                const int d = ch0 + wid;
                const float D = bf2f(ssmD[d]);
                u32x4 zw[4];
#pragma unroll
                for (int c = 0; c < 4; ++c) {
                    const u32x4 uu = *(LAS u32x4*)(cb + A2_U + ((3 - g) >> 1) * A2_UH + (16 * c + i + 15) * 32 + ((3 - g) & 1) * 16);
                    float z[8];
#pragma unroll
                    for (int e = 0; e < 8; ++e) { const int ep = 7 - e; const float u = bf2f((uu[ep >> 1] >> (16 * (ep & 1))) & 0xffff);
                        z[e] = rbf(acc[c][e >> 2][e & 3]) + rbf(u * D); }
                    zw[c] = (u32x4){cvtpk(z[0], z[1]), cvtpk(z[2], z[3]), cvtpk(z[4], z[5]), cvtpk(z[6], z[7])};
                }
                asm volatile("s_waitcnt vmcnt(1)" : "+v"(zw[0]), "+v"(zw[1]), "+v"(zw[2]), "+v"(zw[3])::"memory");
                const rsrc_t rZ = mkrsrc(zT + (size_t)d * S, S * 2);
#pragma unroll
                for (int c = 0; c < 4; ++c) __builtin_amdgcn_raw_buffer_store_b128(zw[c], rZ, (unsigned)(32 * (16 * c + i) + 8 * g) * 2u, 0, 0);
            } else WAIT_V(1);
            WAIT_L(0);
        }
        STAMP(5);
        const int row = wid * 32 + r32;
        const int xb = row * 128 + ((hi ^ ((row >> 1) & 7)) << 4);
        const int wb = 32768 + 8 * (lane & 3) + 64 * ((lane >> 2) & 3) + 32 * ((lane >> 4) & 1) + 2048 * hi;
        f32x16 qa[4] = {};
        asm volatile("" : "+v"(qa[0]), "+v"(qa[1]), "+v"(qa[2]), "+v"(qa[3]));
        const int wl = wid * 10 + lane;
        unsigned wo = wid < 7 ? ((lane < 10 && wl < 64) ? (unsigned)(64 * j32 + wl) * 1280u + 512u : 0x80000000u)
                              : (lane < 16 ? (unsigned)(256 + 2 * j32 + (lane >> 3)) * 8192u + (unsigned)(b & 7) * 1024u + (unsigned)(lane & 7) * 128u : 0x80000000u);
        const unsigned wostep = wid < 7 ? 128u : 64u * 8192u;
        const rsrc_t rWarm = wid < 7 ? rX : rW;
        unsigned vx4[4];
#pragma unroll
        for (int p = 0; p < 4; ++p) vx4[p] = vx[p & 1] + p * (8u * 1280u - 1024u);
        bf16x8 wf[16 + PFD], xf[6];
#define QFRAG(j, so_, jj) do { if (((jj) & 3) == 0) xf[(j) >> 2] = ldsfrag(lds + (so_) + (xb ^ (((jj) >> 2) << 5))); \
                      wf[j] = cat8(trread(lds + (so_) + wb + 4096 * ((jj) >> 2) + 512 * ((jj) & 3)), trread(lds + (so_) + wb + 4096 * ((jj) >> 2) + 512 * ((jj) & 3) + 256)); } while (0)
#define QTILE(KIND, PRE, t, st) do { \
            if (KIND == 1 || KIND == 2) WAIT_V(1); \
            BAR(); \
            if (PHOFF) { PHM0(t); PH(0); } \
            const int so = (st) * 49152, son = ((st) == 2 ? 0 : (st) + 1) * 49152; \
            const unsigned sb2 = ldsb + ((st) == 0 ? 2 : (st) - 1) * 49152u;     \
            const unsigned sxt = sx0 + (unsigned)((t) + 2) * 128u, swt = sw0 + (unsigned)((t) + 2) * 64u * 8192u; \
            if (!(PRE)) { _Pragma("unroll") for (int j = 0; j < PFD; ++j) QFRAG(j, so, j); } \
            else { _Pragma("unroll") for (int j = 0; j < PFD; ++j) wf[j] = wf[16 + j]; xf[0] = xf[4]; xf[1] = xf[5]; } \
            SBAR(); \
            _Pragma("unroll") for (int j = 0; j < 16; ++j) { \
                mfma_acc(qa[j & 3], wf[j], xf[j >> 2]); \
                if (j + PFD < 16) QFRAG(j + PFD, so, j + PFD); else if (KIND != 3) QFRAG(j + PFD, son, j + PFD - 16); \
                if (KIND == 0) { if (j < 6) QPIECE(j, 1, ldsb); else if (j < 12) QPIECE(j - 6, 2, ldsb + 49152u); } \
                if (KIND == 1) { \
                    if (j == 0) { setm0(sb2 + wid * 4096u); dma16o<0>(rX, vx4[0], sxt); } \
                    if (j == 1) dma16o<1024>(rX, vx4[1], sxt); if (j == 2) dma16o<2048>(rX, vx4[2], sxt); if (j == 3) dma16o<3072>(rX, vx4[3], sxt); \
                    if (j == 4) { setm0(sb2 + 32768u + wid * 2048u); dma16o<0>(rW, vw, swt); } \
                    if (j == 5) dma16o<1024>(rW, vw, swt - 896u); } \
                if (KIND == 2) { if (j == 0) dma16x2(rK, vlin, wid * 2048u, ldsb + L_K + wid * 2048u); if (j == 2) dma16x2(rK, vlin, 16384u + wid * 2048u, ldsb + L_K + 16384u + wid * 2048u); } \
                if (KIND == 3) { if (j == 0) dma16x2(rV, vlin, wid * 2048u, ldsb + L_V + wid * 2048u); } \
                if (KIND <= 1 && j == 12) { warm_line(dummy, rWarm, wo); wo = (t) < 5 ? wo + wostep : 0x80000000u; } \
                SBAR(); \
            } } while (0)
        PHINIT();
        QTILE(0, false, 0, 2);
        QTILE(1, false, 1, 0);
        { int st = 1;
          for (int t = 2; t < 8; ++t) { QTILE(1, true, t, st); st = st == 2 ? 0 : st + 1; } }
        QTILE(2, true, 8, 1);
        QTILE(3, true, 9, 2);
#undef QTILE
#undef QFRAG
#undef QPIECE
        asm volatile("s_nop 7\n\ts_nop 7" ::: "memory");
#pragma unroll
        for (int s = 0; s < 8; ++s) {
            u32x4 w;
#pragma unroll
            for (int e = 0; e < 4; ++e) w[e] = cvtpk(qa[s >> 1][8 * (s & 1) + 2 * e] * QSCALE, qa[s >> 1][8 * (s & 1) + 2 * e + 1] * QSCALE);
            q[s] = __builtin_bit_cast(bf16x8, w);
        }
        WAIT_L(0); BAR();
        if (!grp) {
            const lptr dst = lds + L_PARK + wq4 * 8192 + lane * 16;
#pragma unroll
            for (int s = 0; s < 8; ++s) *(LAS bf16x8*)(dst + s * 1024) = q[s];
        }
        WAIT_L(0);
        WAIT_V(0);
        BAR();
    }
    STAMP(1);
    const int kl = L_K + hi * 1024 + r32 * 16, vl = L_V + 8 * (lane & 3) + 64 * ((lane >> 2) & 3) + 32 * ((lane >> 4) & 1) + 256 * hi;
    const unsigned kdst = ldsb + L_K + wq4 * 4096, vdst = ldsb + L_V + wq4 * 4096;
#define TILE(s_) ((s_) <= x2 + 1 ? (s_) : 2 * (s_) - x2 - 2 + grp)
    f32x16 o[4] = {};
    f32x16 s0, s1, negm = {};
    asm volatile("" : "+v"(negm), "+v"(o[0]), "+v"(o[1]), "+v"(o[2]), "+v"(o[3]));
    float mref = 0.f, lsum = 0.f, lsumA = 0.f;
    bf16x8 fr_[32], pp[4];
    int kb = kl, vb = vl;
    int qrow = (grp ? blkA : blkB) * 128 + wq4 * 32;
    const unsigned kmask = rfl(grp ? (0x7fffu >> x2) << x2 : 0x7fffu), vmask = rfl(grp ? 0xffffu : (0xffffu >> (x2 + 1)) << (x2 + 1)), spmask = rfl(1u | (15u << x2) | 0x10000u);
    unsigned ks1 = 16384u + wq4 * 4096u, ks2 = rfl((unsigned)TILE(2) * 16384u + wq4 * 4096u), ko0 = 0u, ko1 = 16384u, ko2 = rfl(((unsigned)TILE(2) & 3u) * 16384u);
#define ROLL(s_) do { const unsigned t3 = (unsigned)TILE((s_) + 2) << 14; ko0 = ko1; ko1 = ko2; ks1 = ks2; ks2 = t3 + wq4 * 4096u; ko2 = t3 & 0xc000u; \
        asm volatile("" : "+s"(ks1), "+s"(ks2), "+s"(ko0), "+s"(ko1), "+s"(ko2)); } while (0)
#define FRAG(i) ((i) < 16 ? ldsfrag(lds + kb + ((i) >> 1) * 2048 + ((i) & 1) * 512) \
                          : cat8(trread(lds + vb + (((i) - 16) >> 2) * 4096 + ((i) & 3) * 512), trread(lds + vb + (((i) - 16) >> 2) * 4096 + ((i) & 3) * 512 + 2048)))
#define QK(LAST, NEXT) do { _Pragma("unroll") for (int i = 0; i < 16; ++i) { \
            if (i < 2) mfma_init(i ? s1 : s0, fr_[i], q[0], negm); else mfma_acc((i & 1) ? s1 : s0, fr_[i], q[i >> 1]); \
            if (i + PFD < (LAST)) fr_[i + PFD] = FRAG(i + PFD); \
            if ((NEXT) >= 0 && i == 4) ROLL(NEXT); \
            SBAR(); } } while (0)
#define PV() do { _Pragma("unroll") for (int i = 0; i < 16; ++i) { \
            mfma_acc(o[i & 3], fr_[16 + i], pp[i >> 2]); \
            if (i + PFD < 16) fr_[16 + i + PFD] = FRAG(16 + i + PFD); \
            SBAR(); } } while (0)
#define EXPSUM() do { float la = 0.f, lb = 0.f; \
            _Pragma("unroll") for (int ks = 0; ks < 4; ++ks) { u32x4 w; \
                _Pragma("unroll") for (int e = 0; e < 4; ++e) { const int r = 8 * (ks & 1) + 2 * e; \
                    const float ea = __builtin_amdgcn_exp2f((ks >> 1) ? s1[r] : s0[r]), eb = __builtin_amdgcn_exp2f((ks >> 1) ? s1[r + 1] : s0[r + 1]); \
                    la = (ks | e) ? fadd_s(la, ea) : ea; lb = (ks | e) ? fadd_s(lb, eb) : eb; w[e] = cvtpk(ea, eb); } \
                pp[ks] = __builtin_bit_cast(bf16x8, w); } \
            tsum = la + lb; } while (0)
#define ROWMAX(rm) do { float ma = max3f(s0[0], s0[1], s1[0]), mb = max3f(s0[2], s0[3], s1[1]); ma = max3f(ma, s1[2], s1[3]); \
            _Pragma("unroll") for (int r = 4; r < 16; r += 4) { ma = max3f(ma, s0[r], s0[r + 1]); mb = max3f(mb, s0[r + 2], s0[r + 3]); ma = max3f(ma, s1[r], s1[r + 1]); mb = max3f(mb, s1[r + 2], s1[r + 3]); } \
            rm = max2f(ma, mb); \
            auto rr_ = __builtin_amdgcn_permlane32_swap(__float_as_uint(rm), __float_as_uint(rm), false, false); rm = max2f(__uint_as_float(rr_[0]), __uint_as_float(rr_[1])); } while (0)
#define STORE_A() do { const lptr tb = lds + L_PARK + wq4 * 8192; \
            u16* cb = ctx + (size_t)(blkA * 128 + wq4 * 32 + (lane >> 4)) * 4096 + head * 128 + (lane & 15) * 8; \
            _Pragma("unroll") for (int i = 0; i < 8; ++i) { const int rr = 4 * i + (lane >> 4); \
                *(u32x4*)(cb + (size_t)i * 4 * 4096) = *(LAS u32x4*)(tb + rr * 256 + (((lane & 15) ^ (rr & 15)) << 4)); } } while (0)
#define VPHASE(LASTT) do { \
            if ((kmask >> s) & 1u) dma16x4(rK, vlin, ks2, kdst + ko2); \
            if ((vmask >> s) & 1u) dma16x4(rV, vlin, ks1, vdst + ko1); \
            const bool sp = (spmask >> s) & 1u; \
            if (__builtin_expect(sp, 0)) { \
                asm volatile("s_nop 7\n\ts_nop 7" ::: "memory"); \
                bool first = s == 0; \
                if (grp && s == x2 + 2) {     \
                    const float rl = __builtin_amdgcn_rcpf(lsumA); \
                    const lptr tb = lds + L_PARK + wq4 * 8192; \
                    _Pragma("unroll") for (int c = 0; c < 8; ++c) { const int d = c >> 1, r = (c & 1) * 8; u32x4 w; \
                        w.x = cvtpk(o[d][r] * rl, o[d][r + 1] * rl); w.y = cvtpk(o[d][r + 2] * rl, o[d][r + 3] * rl); w.z = cvtpk(o[d][r + 4] * rl, o[d][r + 5] * rl); w.w = cvtpk(o[d][r + 6] * rl, o[d][r + 7] * rl); \
                        *(LAS u32x4*)(tb + r32 * 256 + (((2 * c + hi) ^ (r32 & 15)) << 4)) = w; } \
                    _Pragma("unroll") for (int d = 0; d < 4; ++d) o[d] = (f32x16){}; \
                    first = true; \
                } \
                if (grp && s == x2 + 3) STORE_A(); \
                const int rel = qrow - 64 * TILE(s);     \
                if (rel <= 32) { const int th = r32 - 4 * hi; \
                    if (rel == 32) { _Pragma("unroll") for (int r = 0; r < 16; ++r) if ((r & 3) + 8 * (r >> 2) > th) s1[r] = -INFINITY; } \
                    else { if (rel == 0) { _Pragma("unroll") for (int r = 0; r < 16; ++r) if ((r & 3) + 8 * (r >> 2) > th) s0[r] = -INFINITY; } \
                           else { _Pragma("unroll") for (int r = 0; r < 16; ++r) s0[r] = -INFINITY; } \
                           _Pragma("unroll") for (int r = 0; r < 16; ++r) s1[r] = -INFINITY; } } \
                if (first) {     \
                    float rm; ROWMAX(rm); \
                    mref = __builtin_fmaxf(rm, -1e30f); \
                    _Pragma("unroll") for (int r = 0; r < 16; ++r) { s0[r] -= mref; s1[r] -= mref; } \
                    bcast16(negm, hi ? 0.f : 1.f, -mref); \
                } \
            } \
            float tsum; \
            EXPSUM(); \
            if (__builtin_expect(__any(!(tsum <= SUMTHR)), 0)) {     \
                float rm; ROWMAX(rm); \
                const float dl = __builtin_fmaxf(rm, 0.f), f = __builtin_amdgcn_exp2f(-dl); \
                mref += dl; lsum *= f; \
                _Pragma("unroll") for (int r = 0; r < 16; ++r) { s0[r] -= dl; s1[r] -= dl; } \
                bcast16(negm, hi ? 0.f : 1.f, -mref); \
                _Pragma("unroll") for (int d = 0; d < 4; ++d) _Pragma("unroll") for (int r = 0; r < 16; ++r) o[d][r] *= f; \
                EXPSUM(); \
            } \
            lsum += tsum; \
            if (__builtin_expect(sp, 0)) { if (grp && s == x2 + 1) {     \
                auto rr = __builtin_amdgcn_permlane32_swap(__float_as_uint(lsum), __float_as_uint(lsum), false, false); lsumA = __uint_as_float(rr[0]) + __uint_as_float(rr[1]); \
                _Pragma("unroll") for (int u = 0; u < 8; ++u) q[u] = ldsfrag(lds + L_PARK + wq4 * 8192 + lane * 16 + u * 1024); \
                bcast16(negm, 0.f, 0.f); mref = 0.f; lsum = 0.f; qrow = blkB * 128 + wq4 * 32; \
                WAIT_L(0); } } \
            asm volatile("" : "+v"(pp[0]), "+v"(pp[1]), "+v"(pp[2]), "+v"(pp[3]), "+v"(lsum)); \
            SBAR(); \
            vb = vl + ko0; kb = kl + ko1; \
            _Pragma("unroll") for (int u = 0; u < PFD; ++u) fr_[((LASTT) ? 16 : 0) + u] = FRAG(((LASTT) ? 16 : 0) + u); \
            } while (0)
    if (grp) BAR();
    {
#pragma unroll
        for (int u = 0; u < PFD; ++u) fr_[u] = FRAG(u);
        SBAR();
        __builtin_amdgcn_s_setprio(MPRIO);
        QK(16, -1);
        __builtin_amdgcn_s_setprio(0);
    }
    int s = 0;
    PHINIT();
    for (; s < 16; ++s) {
        WAIT_V(0); BAR();
        if (!PHOFF) { PHM0(4 * s + 0); PH(0); }
        VPHASE(false);
        if (!PHOFF) { PHM0(4 * s + 1); PH(1); }
        BAR();
        if (!PHOFF) { PHM0(4 * s + 2); PH(2); }
        __builtin_amdgcn_s_setprio(MPRIO);
        QK(32, s + 1);
        PV();
        __builtin_amdgcn_s_setprio(0);
        if (!PHOFF) { PHM0(4 * s + 3); PH(3); }
    }
    WAIT_V(0); BAR();
    VPHASE(true);
    BAR();
    __builtin_amdgcn_s_setprio(MPRIO);
    PV();
    __builtin_amdgcn_s_setprio(0);
    if (grp && x2 + 3 > 16) { WAIT_L(0); STORE_A(); }
    asm volatile("s_nop 7\n\ts_nop 7" ::: "memory");
#undef FRAG
#undef VPHASE
#undef EXPSUM
#undef ROWMAX
#undef STORE_A
#undef QK
#undef PV
#undef ROLL
#undef TILE
    STAMP(2);
    { auto rr = __builtin_amdgcn_permlane32_swap(__float_as_uint(lsum), __float_as_uint(lsum), false, false); lsum = __uint_as_float(rr[0]) + __uint_as_float(rr[1]); }
    if (!grp) BAR();
    BAR();
    LAS f32x2* ml = (LAS f32x2*)(lds + 65536);
    if (hi == 0) ml[wid * 32 + r32] = (f32x2){mref, lsum};
#define MERGE(KEEP, SEND) do { \
    { const lptr dst = lds + wid * 8192 + lane * 16; \
      _Pragma("unroll") for (int e = 0; e < 8; ++e) { const int d = SEND + (e >> 2), r = (e & 3) * 4; \
          *(LAS f32x4*)(dst + e * 1024) = (f32x4){o[d][r], o[d][r + 1], o[d][r + 2], o[d][r + 3]}; } } \
    WAIT_L(0); BAR(); \
    const f32x2 oth = ml[(wid ^ 4) * 32 + r32]; \
    const float mm = __builtin_fmaxf(mref, oth.x), w0 = __builtin_amdgcn_exp2f(mref - mm), w1 = __builtin_amdgcn_exp2f(oth.x - mm); \
    const float rl = __builtin_amdgcn_rcpf(lsum * w0 + oth.y * w1), sc0 = w0 * rl, sc1 = w1 * rl; \
    { const lptr src = lds + (wid ^ 4) * 8192 + lane * 16; \
      u32x4 w[4]; \
      _Pragma("unroll") for (int c = 0; c < 4; ++c) {     \
          const int d = KEEP + (c >> 1), r = (c & 1) * 8; \
          const f32x4 a0 = *(LAS f32x4*)(src + (2 * c) * 1024), a1 = *(LAS f32x4*)(src + (2 * c + 1) * 1024); \
          w[c].x = cvtpk(o[d][r] * sc0 + a0[0] * sc1, o[d][r + 1] * sc0 + a0[1] * sc1); w[c].y = cvtpk(o[d][r + 2] * sc0 + a0[2] * sc1, o[d][r + 3] * sc0 + a0[3] * sc1); \
          w[c].z = cvtpk(o[d][r + 4] * sc0 + a1[0] * sc1, o[d][r + 5] * sc0 + a1[1] * sc1); w[c].w = cvtpk(o[d][r + 6] * sc0 + a1[2] * sc1, o[d][r + 7] * sc0 + a1[3] * sc1); } \
        \
      WAIT_L(0); \
      const lptr tb = lds + (wid ^ 4) * 8192; \
      _Pragma("unroll") for (int c = 0; c < 4; ++c) *(LAS u32x4*)(tb + r32 * 128 + (((2 * c + hi) ^ (r32 & 7)) << 4)) = w[c]; \
      WAIT_L(0); \
      u16* cb = ctx + (size_t)(blkB * 128 + wq4 * 32 + (lane >> 3)) * 4096 + head * 128 + KEEP * 32 + (lane & 7) * 8; \
      _Pragma("unroll") for (int i = 0; i < 4; ++i) { const int rr = 8 * i + (lane >> 3); \
          *(u32x4*)(cb + (size_t)i * 8 * 4096) = *(LAS u32x4*)(tb + rr * 128 + (((lane & 7) ^ (rr & 7)) << 4)); } } } while (0)
    if (grp) MERGE(2, 0); else MERGE(0, 2);
#undef MERGE
    STAMP(3);
    STAMP_OUT();
}

#define EXP_K3 0
constexpr int K3_STAGE = 73728, K3_LDS = 2 * K3_STAGE;
__global__ __launch_bounds__(512, 2) void k3_oproj(const u16* __restrict__ ctx, const u16* __restrict__ wo, float* __restrict__ part DBGARG) {
    STAMP_DECL; STAMP(0);
    extern __shared__ __attribute__((aligned(16))) char smem[];
    const lptr lds = (lptr)smem;
    const unsigned ldsb = (unsigned)(uintptr_t)smem;
    const int tid = threadIdx.x, lane = tid & 63, fr = lane & 15, fq = lane >> 4;
    const int wid = rfl(tid >> 6), kg = wid >> 2, wm = (wid >> 1) & 1, wn = wid & 1;
    const int b = blockIdx.x;
    const int pm = (b & 7) * 2 + ((b >> 3) & 1), pn = (b >> 4) & 3, ksp = b >> 6;
    const rsrc_t rA = mkrsrc(ctx, S * 4096 * 2), rB = mkrsrc(wo, 4096 * DM * 2);
    unsigned voffA;
    { int R, C; stage_rc(lane * 16, R, C); voffA = (unsigned)R * 8192u + (unsigned)C * 2u; }
    unsigned voffB[5];
#pragma unroll
    for (int j = 0; j < 5; ++j) { const int pc = (wid * 5 + j) % 20, idx = pc * 64 + lane, row = idx / 20, pos = idx % 20, ch = pos ^ (((row >> 3) & 1) << 1);
        voffB[j] = (unsigned)row * 1280u + (unsigned)ch * 16u; }
    const unsigned sA0 = (EXP_K3 ? 0u : (unsigned)pm * 128u * 8192u + (unsigned)ksp * 2048u) + (unsigned)kg * 128u, sB0 = ((EXP_K3 ? 0u : (unsigned)ksp * 1024u) + (unsigned)kg * 64u) * 1280u + (EXP_K3 ? 0u : (unsigned)pn * 320u);
#define ISSUE(t, buf) do { const unsigned sb_ = ldsb + (buf) * K3_STAGE; \
        _Pragma("unroll") for (int j = 0; j < 4; ++j) { const int p = (wid & 3) * 4 + j; dma16(rA, voffA, sA0 + (unsigned)(t) * 256u + (unsigned)(p >> 1) * 16u * 8192u + (p & 1) * 64u, sb_ + kg * 16384 + p * 1024); } \
        _Pragma("unroll") for (int j = 0; j < 5; ++j) dma16(rB, voffB[j], sB0 + (unsigned)(t) * 128u * 1280u, sb_ + 32768 + (wid * 5 + j) * 1024); } while (0)
    const int aoff = kg * 16384 + lds_byte(wm * 64 + fr, fq * 8);
    int bo[2];
    { const int g = lane >> 4, q = (lane >> 2) & 3, p = lane & 3;
      const int base = 32768 + kg * 20480 + (8 * g + q) * 320 + (10 * wn + (p >> 1)) * 16 + 8 * (p & 1);
      const int d = (g & 1) ? 32 : 0;
      bo[0] = base + (wn ? -d : d); bo[1] = base + (wn ? d : -d); }
    f32x4 acc[4][5] = {};
    ISSUE(0, 0);
    for (int t = 0; t < 8; ++t) {
        WAIT_V(0); BAR();
        if (t == 0) STAMP(1);
        if (t + 1 < 8) ISSUE(t + 1, (t + 1) & 1);
        const lptr sb = lds + (t & 1) * K3_STAGE;
#pragma unroll
        for (int ks = 0; ks < 2; ++ks) {
            bf16x8 af[4], bfr[5];
#pragma unroll
            for (int m = 0; m < 4; ++m) af[m] = ldsfrag(sb + aoff + m * 2048 + ks * 1024);
#pragma unroll
            for (int nb = 0; nb < 5; ++nb) bfr[nb] = cat8(trread(sb + bo[nb & 1] + nb * 32 + ks * 10240), trread(sb + bo[nb & 1] + nb * 32 + ks * 10240 + 1280));
#pragma unroll
            for (int m = 0; m < 4; ++m)
#pragma unroll
                for (int nb = 0; nb < 5; ++nb) acc[m][nb] = MFMA16(bfr[nb], af[m], acc[m][nb]);
        }
    }
    BAR();
    STAMP(2);
#define RED(KEEP, SEND) do { \
    { const lptr dst = lds + wid * 10240 + lane * 16; \
      _Pragma("unroll") for (int e = 0; e < 10; ++e) *(LAS f32x4*)(dst + e * 1024) = acc[SEND + e / 5][e % 5]; } \
    WAIT_L(0); BAR(); \
    { const lptr src = lds + (wid ^ 4) * 10240 + lane * 16; \
      float* P = part + (size_t)ksp * S * DM + (size_t)(pm * 128 + wm * 64 + fr) * DM + pn * 160 + wn * 80 + fq * 4; \
      _Pragma("unroll") for (int e = 0; e < 10; ++e) { const int m = KEEP + e / 5, nb = e % 5; \
          *(f32x4*)(P + (size_t)m * 16 * DM + nb * 16) = acc[m][nb] + *(LAS f32x4*)(src + e * 1024); } } } while (0)
    if (kg) RED(2, 0); else RED(0, 2);
#undef RED
    STAMP(3); STAMP_OUT();
#undef ISSUE
}

__global__ __launch_bounds__(320) void a1_kv(const u16* __restrict__ k, const u16* __restrict__ v, const u16* __restrict__ shk, u16* __restrict__ ur) {
    __shared__ __attribute__((aligned(16))) char T[40 * 272];
    const int tid = threadIdx.x, t0 = (blockIdx.x >> 4) * 128, d0 = (blockIdx.x & 15) * 40;
    const int c5 = tid % 5, tp = tid / 5, t = t0 + 2 * tp, d = d0 + 8 * c5;
    u32x4 kr[5], vr[2];
#pragma unroll
    for (int j = 0; j < 5; ++j) { const int tt = t - 3 + j; kr[j] = tt >= 0 ? *(const u32x4*)(k + (size_t)tt * DM + d) : (u32x4){0, 0, 0, 0}; }
#pragma unroll
    for (int j = 0; j < 2; ++j) vr[j] = *(const u32x4*)(v + (size_t)(t + j) * DM + d);
#pragma unroll
    for (int e = 0; e < 8; ++e) {
        const u32x2 sw = *(const u32x2*)(shk + (size_t)(d + e) * 4);
        const float w[4] = {bf2f(sw.x & 0xffff), bf2f(sw.x >> 16), bf2f(sw.y & 0xffff), bf2f(sw.y >> 16)};
        float kk[5];
#pragma unroll
        for (int j = 0; j < 5; ++j) kk[j] = bf2f((kr[j][e >> 1] >> (16 * (e & 1))) & 0xffff);
        float o[2];
#pragma unroll
        for (int j = 0; j < 2; ++j) {
            float s = rbf(kk[j] * w[3]);
            s = rbf(s + rbf(kk[j + 1] * w[2])); s = rbf(s + rbf(kk[j + 2] * w[1])); s = rbf(s + rbf(kk[j + 3] * w[0]));
            o[j] = s * bf2f((vr[j][e >> 1] >> (16 * (e & 1))) & 0xffff);
        }
        *(unsigned*)(T + (8 * c5 + e) * 272 + (126 - 2 * tp) * 2) = cvtpk(o[1], o[0]);
    }
    __syncthreads();
    for (int i = tid; i < 640; i += 320) { const int row = i >> 4, ch = i & 15;
        *(u32x4*)(ur + (size_t)(d0 + row) * S + (1920 - t0) + ch * 8) = *(const u32x4*)(T + row * 272 + ch * 16); }
}

__global__ __launch_bounds__(512) void k4_mix(const u16* __restrict__ x, const u16* __restrict__ gw, const u16* __restrict__ zT, const float* __restrict__ part, u16* __restrict__ out) {
    __shared__ __attribute__((aligned(16))) u16 zs[8][DM + 8];
    const int tid = threadIdx.x, lane = tid & 63, w = tid >> 6, t0 = blockIdx.x * 8, t = t0 + w;
    for (int d = tid; d < DM; d += 512) { const u32x4 zz = *(const u32x4*)(zT + (size_t)d * S + t0);
#pragma unroll
        for (int e = 0; e < 8; ++e) zs[e][d] = (u16)((zz[e >> 1] >> (16 * (e & 1))) & 0xffff); }
    u32x4 xq[2]; f32x4 bs[2][2];
    float gs = 0.f;
#pragma unroll
    for (int p = 0; p < 2; ++p) {
        const int ch = p * 64 + lane;
        if (ch < 80) {
            xq[p] = *(const u32x4*)(x + (size_t)t * DM + ch * 8);
            const u32x4 gg = *(const u32x4*)(gw + ch * 8);
            const float* P = part + (size_t)t * DM + ch * 8;
#pragma unroll
            for (int hh = 0; hh < 2; ++hh) bs[p][hh] = (*(const f32x4*)(P + hh * 4) + *(const f32x4*)(P + (size_t)S * DM + hh * 4)) + (*(const f32x4*)(P + (size_t)2 * S * DM + hh * 4) + *(const f32x4*)(P + (size_t)3 * S * DM + hh * 4));
#pragma unroll
            for (int e = 0; e < 8; ++e) gs += rbf(bf2f((xq[p][e >> 1] >> (16 * (e & 1))) & 0xffff) * bf2f((gg[e >> 1] >> (16 * (e & 1))) & 0xffff));
        }
    }
#pragma unroll
    for (int o = 32; o >= 1; o >>= 1) gs += __shfl_xor(gs, o);
    const float g = rbf(1.f / (1.f + __expf(-rbf(gs)))), g1 = rbf(1.f - g);
    __syncthreads();
#pragma unroll
    for (int p = 0; p < 2; ++p) {
        const int ch = p * 64 + lane;
        if (ch < 80) {
            const u32x4 zz = *(const u32x4*)(&zs[w][ch * 8]);
            float o[8];
#pragma unroll
            for (int e = 0; e < 8; ++e) {
                const float a = rbf(bf2f((xq[p][e >> 1] >> (16 * (e & 1))) & 0xffff) * bf2f((zz[e >> 1] >> (16 * (e & 1))) & 0xffff));
                o[e] = rbf(g * a) + rbf(g1 * rbf(bs[p][e >> 2][e & 3]));
            }
            u32x4 wv; wv.x = cvtpk(o[0], o[1]); wv.y = cvtpk(o[2], o[3]); wv.z = cvtpk(o[4], o[5]); wv.w = cvtpk(o[6], o[7]);
            *(u32x4*)(out + (size_t)t * DM + ch * 8) = wv;
        }
    }
}

extern "C" void kernel_launch(void* const* d_in, const int* in_sizes, int n_in, void* d_out, int out_size, void* d_ws, size_t ws_size, hipStream_t stream) {
    (void)in_sizes; (void)n_in; (void)out_size; (void)ws_size;
    const u16* Aq = (const u16*)d_in[0]; const u16* Ak = (const u16*)d_in[1]; const u16* Av = (const u16*)d_in[2];
    const u16* shk = (const u16*)d_in[3]; const u16* ssmk = (const u16*)d_in[4]; const u16* ssmD = (const u16*)d_in[5];
    const u16* ckv = (const u16*)d_in[7]; const u16* wq = (const u16*)d_in[8]; const u16* wuk = (const u16*)d_in[9];
    const u16* wuv = (const u16*)d_in[10]; const u16* wo = (const u16*)d_in[11]; const u16* gw = (const u16*)d_in[12];
    char* ws = (char*)d_ws;
    (void)hipFuncSetAttribute((const void*)k1_kvproj, hipFuncAttributeMaxDynamicSharedMemorySize, K1_LDS);
    (void)hipFuncSetAttribute((const void*)k2_attn, hipFuncAttributeMaxDynamicSharedMemorySize, K2_LDS);
    (void)hipFuncSetAttribute((const void*)k3_oproj, hipFuncAttributeMaxDynamicSharedMemorySize, K3_LDS);
    a1_kv<<<256, 320, 0, stream>>>(Ak, Av, shk, (u16*)(ws + WS_UR));
    k1_kvproj<<<256, 512, K1_LDS, stream>>>(ckv, wuk, wuv, ws + WS_K, ws + WS_V DBGPASS(0));
    k2_attn<<<256, 512, K2_LDS, stream>>>(Aq, wq, ws + WS_K, ws + WS_V, (u16*)(ws + WS_CTX), (const u16*)(ws + WS_UR), ssmk, ssmD, (u16*)(ws + WS_ZT) DBGPASS(1));
    k3_oproj<<<256, 512, K3_LDS, stream>>>((const u16*)(ws + WS_CTX), wo, (float*)(ws + WS_PART) DBGPASS(2));
    k4_mix<<<256, 512, 0, stream>>>(Aq, gw, (const u16*)(ws + WS_ZT), (const float*)(ws + WS_PART), (u16*)d_out);
}
```

```cpp
#include <hip/hip_runtime.h>
#include <stdint.h>

#define DI __device__ __forceinline__
#define LAS __attribute__((address_space(3)))
typedef unsigned short u16;
typedef __bf16 bf16x8 __attribute__((ext_vector_type(8)));
typedef __bf16 bf16x2 __attribute__((ext_vector_type(2)));
typedef short s16x4 __attribute__((ext_vector_type(4)));
typedef short s16x8 __attribute__((ext_vector_type(8)));
typedef float f32x2 __attribute__((ext_vector_type(2)));
typedef float f32x4 __attribute__((ext_vector_type(4)));
typedef float f32x16 __attribute__((ext_vector_type(16)));
typedef unsigned u32x4 __attribute__((ext_vector_type(4)));
typedef unsigned u32x2 __attribute__((ext_vector_type(2)));
typedef __amdgpu_buffer_rsrc_t rsrc_t;
typedef LAS char* lptr;

constexpr int S = 2048, DM = 640, NH = 32, HD = 128, DC = 512;
constexpr size_t MB = 1u << 20;
constexpr size_t WS_K = 0, WS_V = 16 * MB, WS_CTX = 32 * MB, WS_WT = 48 * MB, WS_UR = 72 * MB, WS_ZT = 76 * MB, WS_GATE = 80 * MB;

#define SBAR() __builtin_amdgcn_sched_barrier(0)
#define WAIT_V(n) asm volatile("s_waitcnt vmcnt(" #n ")" ::: "memory")
#define WAIT_L(n) asm volatile("s_waitcnt lgkmcnt(" #n ")" ::: "memory")
#define BAR() asm volatile("s_barrier" ::: "memory")
#define MFMA32(a, b, c) __builtin_amdgcn_mfma_f32_32x32x16_bf16((a), (b), (c), 0, 0, 0)
#define MFMA16(a, b, c) __builtin_amdgcn_mfma_f32_16x16x32_bf16((a), (b), (c), 0, 0, 0)

DI unsigned rfl(unsigned x) { return __builtin_amdgcn_readfirstlane(x); }
DI unsigned cvtpk(float lo, float hi) { f32x2 v = {lo, hi}; bf16x2 b = __builtin_convertvector(v, bf16x2); return __builtin_bit_cast(unsigned, b); }
DI float bf2f(unsigned b16) { return __uint_as_float(b16 << 16); }
DI float rbf(float x) { return (float)(__bf16)x; }
DI rsrc_t mkrsrc(const void* p, unsigned bytes) { return __builtin_amdgcn_make_buffer_rsrc((void*)p, (short)0, (int)bytes, 0x00020000); }
DI void dma16(rsrc_t r, unsigned voff, unsigned soff, unsigned lds) {
    asm volatile("s_mov_b32 m0, %0\n\ts_nop 0\n\tbuffer_load_dwordx4 %1, %2, %3 offen lds" ::"s"(lds), "v"(voff), "s"(r), "s"(soff) : "memory");
}
DI void dma16x4(rsrc_t r, unsigned voff, unsigned soff, unsigned lds) {
    asm volatile("s_mov_b32 m0, %0\n\ts_nop 0\n\tbuffer_load_dwordx4 %1, %2, %3 offen lds\n\tbuffer_load_dwordx4 %1, %2, %3 offen offset:1024 lds\n\t"
                 "buffer_load_dwordx4 %1, %2, %3 offen offset:2048 lds\n\tbuffer_load_dwordx4 %1, %2, %3 offen offset:3072 lds" ::"s"(lds), "v"(voff), "s"(r), "s"(soff) : "memory");
}
template <int P, int BIT> DI void dma16p(rsrc_t r, unsigned voff, unsigned soff, unsigned lds, unsigned en) {
    asm volatile("s_bitcmp1_b32 %4, %6\n\ts_cbranch_scc0 1f\n\ts_mov_b32 m0, %0\n\ts_nop 0\n\tbuffer_load_dwordx4 %1, %2, %3 offen offset:%5 lds\n1:" ::"s"(lds), "v"(voff), "s"(r), "s"(soff), "s"(en), "i"(P * 1024), "i"(BIT) : "memory", "scc");
}
DI void dma16x2(rsrc_t r, unsigned voff, unsigned soff, unsigned lds) {
    asm volatile("s_mov_b32 m0, %0\n\ts_nop 0\n\tbuffer_load_dwordx4 %1, %2, %3 offen lds\n\tbuffer_load_dwordx4 %1, %2, %3 offen offset:1024 lds" ::"s"(lds), "v"(voff), "s"(r), "s"(soff) : "memory");
}
DI void setm0(unsigned lds) { asm volatile("s_mov_b32 m0, %0\n\ts_nop 0" ::"s"(lds) : "memory"); }
template <int OFF> DI void dma16o(rsrc_t r, unsigned voff, unsigned soff) { asm volatile("buffer_load_dwordx4 %0, %1, %2 offen offset:%3 lds" ::"v"(voff), "s"(r), "s"(soff), "i"(OFF) : "memory"); }
DI bf16x8 ldsfrag(lptr p) { return *(LAS bf16x8*)p; }
typedef short v4i16_t __attribute__((ext_vector_type(4)));
DI s16x4 trread(lptr p) { return __builtin_amdgcn_ds_read_tr16_b64_v4i16((LAS v4i16_t*)p); }
DI bf16x8 cat8(s16x4 lo, s16x4 hi) { return __builtin_bit_cast(bf16x8, __builtin_shufflevector(lo, hi, 0, 1, 2, 3, 4, 5, 6, 7)); }

#define PH(k) do {} while (0)
#define PHM0(idx) do {} while (0)
#define PHINIT() do {} while (0)
#define STAMP_DECL do {} while (0)
#define STAMP(k) do {} while (0)
#define STAMP_OUT() do {} while (0)
#define DBGARG
#define DBGPASS(off)
DI int lds_byte(int r, int c) { const int st = (r >> 4) * 2 + (c >> 5), rr = r & 15, cc = c & 31, ob = rr * 64 + cc * 2; return st * 1024 + (ob ^ (((ob >> 9) & 1) << 5)); }
DI void stage_rc(int b, int& R, int& C) { const int st = b / 1024, sb = b % 1024, swz = sb ^ (((sb >> 9) & 1) << 5); R = (st >> 1) * 16 + swz / 64; C = (st & 1) * 32 + (swz % 64) / 2; }

#define EXP_K1 0
constexpr int K1_SIDE = 8 * 16384, K1_LDS = K1_SIDE + 22528;
DI void ld16a(u32x4& d, rsrc_t r, unsigned voff) { asm volatile("buffer_load_dwordx4 %0, %1, %2, 0 offen" : "=v"(d) : "v"(voff), "s"(r) : "memory"); }
__global__ __launch_bounds__(512, 2) void k1_kvproj(const u16* __restrict__ ckv, const u16* __restrict__ wuk, const u16* __restrict__ wuv, char* __restrict__ Kout, char* __restrict__ Vout,
                                                    const u16* __restrict__ Ak, const u16* __restrict__ Av, const u16* __restrict__ shk, u16* __restrict__ ur,
                                                    const u16* __restrict__ x, const u16* __restrict__ gw, float* __restrict__ gate,
                                                    const u16* __restrict__ wo, char* __restrict__ wt DBGARG) {
    STAMP_DECL; STAMP(0);
    extern __shared__ __attribute__((aligned(16))) char smem[];
    const lptr lds = (lptr)smem;
    const unsigned ldsb = (unsigned)(uintptr_t)smem;
    const int tid = threadIdx.x, lane = tid & 63, fr = lane & 15, fq = lane >> 4;
    const int wid = rfl(tid >> 6), wr = wid >> 2, wc = wid & 3;
    const int b = blockIdx.x, head = 4 * (b & 7) + (b >> 6), pm = (b >> 3) & 7;
    const rsrc_t rA = mkrsrc(ckv, S * DC * 2), rK = mkrsrc(wuk, DC * 4096 * 2), rV = mkrsrc(wuv, DC * 4096 * 2);
    unsigned voffA, voffB;
    { int R, C; stage_rc(tid * 16, R, C); voffA = (unsigned)(R * DC + C) * 2u;
      const int row = 4 * wid + (lane >> 4), swz = ((row & 3) << 2) | (((row >> 2) & 1) << 1) | ((row >> 3) & 1);
      voffB = (unsigned)row * 8192u + (unsigned)(((lane & 15) ^ swz) * 16); }
    const unsigned sA0 = (EXP_K1 ? 0u : (unsigned)pm * 256u * 1024u), sB0 = (EXP_K1 ? 0u : (unsigned)head * 256u), ldw = ldsb + (unsigned)wid * 1024u;
#define SA(b_, h_) (((b_) * 2 + (h_)) * 16384)
#define SB(b_, h_) ((4 + (b_) * 2 + (h_)) * 16384)
#define STAGE_A(b_, h_, kt) do { dma16(rA, voffA, sA0 + (h_) * 131072u + (unsigned)(kt) * 128u, ldw + SA(b_, h_)); dma16(rA, voffA, sA0 + (h_) * 131072u + (unsigned)(kt) * 128u + 65536u, ldw + SA(b_, h_) + 8192); } while (0)
#define STAGE_B(b_, h_, kt) do { dma16((h_) ? rV : rK, voffB, sB0 + (unsigned)(kt) * 524288u, ldw + SB(b_, h_)); dma16((h_) ? rV : rK, voffB, sB0 + (unsigned)(kt) * 524288u + 262144u, ldw + SB(b_, h_) + 8192); } while (0)
    const int aoff = lds_byte(wr * 64 + fr, fq * 8);
    int boff[2][2];
    { const int g = lane >> 4, q = (lane >> 2) & 3, p = lane & 3;
      const int base = (8 * g + q) * 256 + ((wc ^ q) << 6) + ((p >> 1) << 5) + ((g & 1) << 4) + 8 * (p & 1);
#pragma unroll
      for (int n = 0; n < 2; ++n)
#pragma unroll
          for (int t = 0; t < 2; ++t) boff[n][t] = (base ^ (t << 5) ^ (n << 4)) + t * 1024; }
#define LDA(dst, b_, h_) do { _Pragma("unroll") for (int m = 0; m < 4; ++m) _Pragma("unroll") for (int k = 0; k < 2; ++k) dst[m][k] = ldsfrag(lds + SA(b_, h_) + aoff + m * 2048 + k * 1024); } while (0)
#define LDB(dst, b_, h_) do { _Pragma("unroll") for (int n = 0; n < 2; ++n) _Pragma("unroll") for (int k = 0; k < 2; ++k) \
        dst[n][k] = cat8(trread(lds + SB(b_, h_) + boff[n][0] + k * 8192), trread(lds + SB(b_, h_) + boff[n][1] + k * 8192)); } while (0)
#define MMA(ai, bj, At_, Bt_) do { __builtin_amdgcn_s_setprio(1); _Pragma("unroll") for (int m = 0; m < 4; ++m) _Pragma("unroll") for (int n = 0; n < 2; ++n) _Pragma("unroll") for (int k = 0; k < 2; ++k) \
        acc[ai][bj][m][n] = MFMA16(Bt_[n][k], At_[m][k], acc[ai][bj][m][n]); __builtin_amdgcn_s_setprio(0); } while (0)
    f32x4 acc[2][2][4][2] = {};
    bf16x8 At[4][2], B0[2][2], B1[2][2];
    constexpr int nt = DC / 64;
    {
        const int t0 = (b >> 4) * 128, d0 = (b & 15) * 40;
        const rsrc_t rk = mkrsrc(Ak, S * DM * 2), rv = mkrsrc(Av, S * DM * 2), rs = mkrsrc(shk, DM * 8);
#pragma unroll
        for (int j = 0; j < 3; ++j) { const int pc = wid + 8 * j;
            if (pc < 11) { const int idx = pc * 64 + lane, row = idx / 5, t = t0 - 3 + row; dma16(rk, (t >= 0 && row < 131) ? (unsigned)(t * DM + d0 + (idx % 5) * 8) * 2u : 0x80000000u, 0u, ldsb + K1_SIDE + pc * 1024u); }
            else if (pc < 21) { const int idx = (pc - 11) * 64 + lane; dma16(rv, (unsigned)((t0 + idx / 5) * DM + d0 + (idx % 5) * 8) * 2u, 0u, ldsb + K1_SIDE + pc * 1024u); }
            else if (pc == 21) dma16(rs, lane < 20 ? (unsigned)(d0 * 8 + lane * 16) : 0x80000000u, 0u, ldsb + K1_SIDE + 21504u); }
    }
    STAGE_B(0, 0, 0); STAGE_A(0, 0, 0); STAGE_B(0, 1, 0); STAGE_A(0, 1, 0);
    if (wr == 1) BAR();
    WAIT_V(4); BAR();
    STAGE_B(1, 0, 1); STAGE_A(1, 0, 1); STAGE_B(1, 1, 1);
    WAIT_V(6); BAR();
    STAMP(1);
    for (int t = 0; t < nt - 2; t += 2) {
        LDB(B0, 0, 0); SBAR(); LDA(At, 0, 0); STAGE_A(1, 1, t + 1);
        WAIT_L(8); BAR(); WAIT_L(0); MMA(0, 0, At, B0); BAR(); SBAR();
        LDB(B1, 0, 1); STAGE_B(0, 0, t + 2);
        BAR(); WAIT_L(0); MMA(0, 1, At, B1); BAR();
        LDA(At, 0, 1); STAGE_A(0, 0, t + 2);
        BAR(); WAIT_L(0); MMA(1, 0, At, B0); BAR(); SBAR();
        STAGE_B(0, 1, t + 2);
        WAIT_V(6); BAR(); MMA(1, 1, At, B1); BAR();
        LDB(B0, 1, 0); SBAR(); LDA(At, 1, 0); STAGE_A(0, 1, t + 2);
        WAIT_L(8); BAR(); WAIT_L(0); MMA(0, 0, At, B0); BAR(); SBAR();
        LDB(B1, 1, 1); STAGE_B(1, 0, t + 3);
        BAR(); WAIT_L(0); MMA(0, 1, At, B1); BAR();
        LDA(At, 1, 1); STAGE_A(1, 0, t + 3);
        BAR(); WAIT_L(0); MMA(1, 0, At, B0); BAR(); SBAR();
        STAGE_B(1, 1, t + 3);
        WAIT_V(6); BAR(); MMA(1, 1, At, B1); BAR();
    }
    { LDB(B0, 0, 0); WAIT_V(0); LDA(At, 0, 0); STAGE_A(1, 1, nt - 1);
      BAR(); WAIT_L(0); MMA(0, 0, At, B0); BAR();
      LDB(B1, 0, 1); BAR(); WAIT_L(0); MMA(0, 1, At, B1); BAR();
      LDA(At, 0, 1); WAIT_V(4); BAR(); WAIT_L(0); MMA(1, 0, At, B0); MMA(1, 1, At, B1); BAR(); }
    { LDB(B0, 1, 0); LDA(At, 1, 0); WAIT_V(2); BAR(); WAIT_L(0); MMA(0, 0, At, B0); BAR();
      LDB(B1, 1, 1); WAIT_V(0); BAR(); WAIT_L(0); MMA(0, 1, At, B1); BAR();
      LDA(At, 1, 1); BAR(); WAIT_L(0); MMA(1, 0, At, B0); MMA(1, 1, At, B1); BAR(); }
    if (wr == 0) BAR();
    STAMP(2);
    {
        const rsrc_t rwo = mkrsrc(wo, 4096 * DM * 2), rx = mkrsrc(x, S * DM * 2), rg = mkrsrc(gw, DM * 2);
#pragma unroll
        for (int j = 0; j < 3; ++j) { const int pc = wid + 8 * j, idx = pc * 64 + lane, row = idx / 40, ch = idx % 40;
            dma16(rwo, pc < 20 ? (unsigned)((b >> 1) * 32 + row) * 1280u + (unsigned)(b & 1) * 640u + (unsigned)ch * 16u : 0x80000000u, 0u, ldsb + (pc < 20 ? pc : 20) * 1024u); }
        u32x4 xg[2], gg[2];
#pragma unroll
        for (int j = 0; j < 2; ++j) { const int ch = 64 * j + lane;
            ld16a(xg[j], rx, ch < 80 ? (unsigned)((8 * b + wid) * DM + ch * 8) * 2u : 0x80000000u); ld16a(gg[j], rg, ch < 80 ? (unsigned)ch * 16u : 0x80000000u); }
        STAMP(4);
        char* Kh = Kout + (size_t)head * 524288; char* Vh = Vout + (size_t)head * 524288;
#pragma unroll
        for (int ai = 0; ai < 2; ++ai) {
            const int tile = 4 * pm + 2 * ai + wr;
#pragma unroll
            for (int m = 0; m < 4; ++m) {
                const int kk = 16 * m + fr;
#pragma unroll
                for (int bj = 0; bj < 2; ++bj) {
                    const f32x4 v0 = acc[ai][bj][m][0], v1 = acc[ai][bj][m][1];
                    u32x4 w; w.x = cvtpk(v0[0], v0[1]); w.y = cvtpk(v0[2], v0[3]); w.z = cvtpk(v1[0], v1[1]); w.w = cvtpk(v1[2], v1[3]);
                    if (bj == 0) *(u32x4*)(Kh + tile * 16384 + (4 * wc + fq) * 1024 + kk * 16) = w;
                    else *(u32x4*)(Vh + tile * 16384 + ((kk >> 3) * 4 + wc) * 512 + (kk & 7) * 64 + fq * 16) = w;
                }
            }
        }
        STAMP(5);
        const int t0 = (b >> 4) * 128, d0 = (b & 15) * 40, c5 = tid % 5, tp = tid / 5;
        const lptr T = lds + 32768;
        if (tid < 320) {
            u32x4 kr[5], vr[2], sw[4];
#pragma unroll
            for (int j = 0; j < 5; ++j) kr[j] = *(LAS u32x4*)(lds + K1_SIDE + (2 * tp + j) * 80 + c5 * 16);
#pragma unroll
            for (int j = 0; j < 2; ++j) vr[j] = *(LAS u32x4*)(lds + K1_SIDE + 11264 + (2 * tp + j) * 80 + c5 * 16);
#pragma unroll
            for (int j = 0; j < 4; ++j) sw[j] = *(LAS u32x4*)(lds + K1_SIDE + 21504 + c5 * 64 + j * 16);
#pragma unroll
            for (int e = 0; e < 8; ++e) {
                const unsigned s01 = sw[e >> 1][2 * (e & 1)], s23 = sw[e >> 1][2 * (e & 1) + 1];
                const float w[4] = {bf2f(s01 & 0xffff), bf2f(s01 >> 16), bf2f(s23 & 0xffff), bf2f(s23 >> 16)};
                float kk[5];
#pragma unroll
                for (int j = 0; j < 5; ++j) kk[j] = bf2f((kr[j][e >> 1] >> (16 * (e & 1))) & 0xffff);
                float o[2];
#pragma unroll
                for (int j = 0; j < 2; ++j) {
                    float s = rbf(kk[j] * w[3]);
                    s = rbf(s + rbf(kk[j + 1] * w[2])); s = rbf(s + rbf(kk[j + 2] * w[1])); s = rbf(s + rbf(kk[j + 3] * w[0]));
                    o[j] = s * bf2f((vr[j][e >> 1] >> (16 * (e & 1))) & 0xffff);
                }
                *(LAS unsigned*)(T + (8 * c5 + e) * 272 + (126 - 2 * tp) * 2) = cvtpk(o[1], o[0]);
            }
        }
        STAMP(6);
        asm volatile("s_waitcnt vmcnt(16)" : "+v"(xg[0]), "+v"(xg[1]), "+v"(gg[0]), "+v"(gg[1])::"memory");
        float gs = 0.f;
#pragma unroll
        for (int j = 0; j < 2; ++j)
#pragma unroll
            for (int e = 0; e < 8; ++e) gs += rbf(bf2f((xg[j][e >> 1] >> (16 * (e & 1))) & 0xffff) * bf2f((gg[j][e >> 1] >> (16 * (e & 1))) & 0xffff));
#pragma unroll
        for (int o = 32; o >= 1; o >>= 1) gs += __shfl_xor(gs, o);
        const float gt = rbf(1.f / (1.f + __expf(-rbf(gs))));
        STAMP(7);
        WAIT_L(0); BAR();
        STAMP(8);
        const rsrc_t ru = mkrsrc(ur, DM * S * 2), rgo = mkrsrc(gate, S * 8);
#pragma unroll
        for (int r = 0; r < 2; ++r) { const int i = tid + 512 * r, row = i >> 4, ch = i & 15; const bool v = i < 640;
            const u32x4 w = *(LAS u32x4*)(T + (v ? row : 0) * 272 + ch * 16);
            __builtin_amdgcn_raw_buffer_store_b128(w, ru, v ? (unsigned)((d0 + row) * S + (1920 - t0) + ch * 8) * 2u : 0x80000000u, 0, 0); }
        __builtin_amdgcn_raw_buffer_store_b64((u32x2){__float_as_uint(gt), __float_as_uint(rbf(1.f - gt))}, rgo, lane == 0 ? (unsigned)(8 * b + wid) * 8u : 0x80000000u, 0, 0);
#pragma unroll
        for (int j = 0; j < 3; ++j) { const int nt = wid + 8 * j;
            if (nt < 20) {
                const lptr src = lds + (8 * (lane >> 4) + ((lane >> 2) & 3)) * 640 + nt * 32 + (lane & 3) * 8;
                *(bf16x8*)(wt + ((size_t)(b >> 1) * 40 + (b & 1) * 20 + nt) * 1024 + lane * 16) = cat8(trread(src), trread(src + 4 * 640)); } }
    }
    STAMP(3); STAMP_OUT();
#undef SA
#undef SB
#undef STAGE_A
#undef STAGE_B
#undef LDA
#undef LDB
#undef MMA
}

constexpr int K2_LDS = 163840;
constexpr float QSCALE = 0.08838834764831845f * 1.4426950408889634f;
constexpr float SUMTHR = 256.0f;
constexpr int L_K = 0, L_V = 65536, L_PARK = 131072;
#define MPRIO 1
#define SPREAD 1
#define QEXP 0
#define PHOFF 0
constexpr int PFD = 5;
DI float max3f(float a, float b, float c) { float r; asm("v_max3_f32 %0, %1, %2, %3" : "=v"(r) : "v"(a), "v"(b), "v"(c)); return r; }
DI float max2f(float a, float b) { float r; asm("v_max_f32_e32 %0, %1, %2" : "=v"(r) : "v"(a), "v"(b)); return r; }
DI float fadd_s(float a, float b) { float r = a + b; asm("" : "+v"(r)); return r; }
DI void mfma_init(f32x16& d, bf16x8 a, bf16x8 b, const f32x16& c) { asm volatile("v_mfma_f32_32x32x16_bf16 %0, %1, %2, %3" : "=&v"(d) : "v"(a), "v"(b), "v"(c)); }
DI void mfma_zero(f32x16& d, bf16x8 a, bf16x8 b) { asm volatile("v_mfma_f32_32x32x16_bf16 %0, %1, %2, 0" : "=&v"(d) : "v"(a), "v"(b)); }
DI void mfma_acc(f32x16& d, bf16x8 a, bf16x8 b) { asm volatile("v_mfma_f32_32x32x16_bf16 %0, %1, %2, %0" : "+v"(d) : "v"(a), "v"(b)); }
DI void bcast16(f32x16& d, float aone, float v) { asm volatile("s_nop 1\n\tv_mfma_f32_32x32x2_f32 %0, %1, %2, 0" : "+v"(d) : "v"(aone), "v"(v)); }
DI void warm_line(unsigned ldsdummy, rsrc_t r, unsigned voff) { asm volatile("s_mov_b32 m0, %0\n\ts_nop 0\n\tbuffer_load_dword %1, %2, 0 offen lds" ::"s"(ldsdummy), "v"(voff), "s"(r) : "memory"); }

constexpr int A2_HC = 4160, A2_U = 4 * A2_HC, A2_UH = 79 * 32, A2_CH = A2_U + 2 * A2_UH;
typedef unsigned u32x6 __attribute__((ext_vector_type(6)));

__global__ __launch_bounds__(512, 2) void k2_attn(const u16* __restrict__ x, const u16* __restrict__ wq, const char* __restrict__ Kg, const char* __restrict__ Vg, char* __restrict__ ctxf,
                                                  const u16* __restrict__ ur, const u16* __restrict__ ssmk, const u16* __restrict__ ssmD, u16* __restrict__ zT DBGARG) {
    STAMP_DECL; STAMP(0);
    extern __shared__ __attribute__((aligned(16))) char smem[];
    const lptr lds = (lptr)smem;
    const unsigned ldsb = (unsigned)(uintptr_t)smem;
    const int tid = threadIdx.x, lane = tid & 63, r32 = lane & 31, hi = lane >> 5;
    const int wid = rfl(tid >> 6), grp = wid >> 2, wq4 = wid & 3;
    const int b = blockIdx.x, head = 4 * (b & 7) + (b >> 6), xi = (b >> 3) & 7, j32 = b >> 3;
    const int blkB = 15 - xi, blkA = xi, x2 = 2 * xi;
    const rsrc_t rK = mkrsrc(Kg + (size_t)head * 524288, 524288), rV = mkrsrc(Vg + (size_t)head * 524288, 524288);
    const unsigned vlin = (unsigned)lane * 16u;
    bf16x8 q[8];
    {
        const rsrc_t rX = mkrsrc(x, S * DM * 2), rW = mkrsrc(wq, DM * 4096 * 2);
        unsigned vx[2];
#pragma unroll
        for (int j = 0; j < 2; ++j) vx[j] = (unsigned)(lane >> 3) * 1280u + (unsigned)(((lane & 7) ^ (4 * j + (lane >> 4))) * 16);
        const unsigned vw = (unsigned)((lane >> 2) & 7) * 8192u + (unsigned)(lane >> 5) * 64u + (unsigned)(lane & 3) * 16u;
        const unsigned sx0 = (unsigned)((grp ? blkA : blkB) * 128 + wq4 * 32) * 1280u, sw0 = (unsigned)wid * 8u * 8192u + (unsigned)head * 256u;
#define QPIECE(p, t, sb_) do { if ((p) < 4) dma16(rX, vx[(p) & 1], sx0 + (p) * 8u * 1280u + (unsigned)(t) * 128u, (sb_) + wid * 4096u + (p) * 1024u); \
                               else dma16(rW, vw, sw0 + (unsigned)(t) * 64u * 8192u + ((p) & 1) * 128u, (sb_) + 32768u + (wid * 2 + ((p) & 1)) * 1024u); } while (0)
        const unsigned dummy = ldsb + 147456u;
        {
            const int nch = b < 128 ? 3 : 2, ch0 = b < 128 ? 3 * b : 384 + 2 * (b - 128);
            const rsrc_t rH = mkrsrc(ssmk + (size_t)ch0 * S, (unsigned)nch * S * 2), rU = mkrsrc(ur + (size_t)ch0 * S, (unsigned)nch * S * 2);
            u32x4 hlo[2], hhi[2];
#pragma unroll
            for (int rd = 0; rd < 2; ++rd) {
                const int task = tid + 512 * rd, c = task / 260, ck = task - 260 * c, n0 = 8 * ck - 32;
                const bool on = c < nch;
                ld16a(hlo[rd], rH, (on && n0 >= 0) ? (unsigned)(c * S + n0) * 2u : 0x80000000u);
                ld16a(hhi[rd], rH, (on && n0 + 8 >= 0 && n0 + 8 < S) ? (unsigned)(c * S + n0 + 8) * 2u : 0x80000000u);
            }
            if (wid < nch) {
#pragma unroll
                for (int p = 0; p < 4; ++p) { const int J = 32 * (p & 1) + (lane >> 1), g = 2 * (p >> 1) + (lane & 1);
                    dma16(rU, (unsigned)(4 * (63 - J) + g) * 16u, (unsigned)wid * (S * 2u), ldsb + wid * A2_CH + A2_U + (p >> 1) * A2_UH + (15 + 32 * (p & 1)) * 32); }
            }
#pragma unroll
            for (int p = 0; p < 6; ++p) QPIECE(p, 0, ldsb + 2 * 49152u);
            {
                const int i = tid - 192, l = 48 * j32 + i;
                const unsigned ox = (unsigned)(64 * j32 + (tid & 63)) * 1280u + (unsigned)(1 + (tid >> 6)) * 128u;
                const unsigned ow = (unsigned)(64 + (l >> 3)) * 8192u + (unsigned)(b & 7) * 1024u + (unsigned)(l & 7) * 128u;
                if (wid < 3) warm_line(dummy, rX, ox); else warm_line(dummy, rW, tid < 240 ? ow : 0x80000000u);
            }
            if (wid < nch && lane < 60) *(LAS u32x4*)(lds + wid * A2_CH + A2_U + (lane / 30) * A2_UH + (lane % 30) * 16) = (u32x4){0, 0, 0, 0};
            asm volatile("s_waitcnt vmcnt(7)" : "+v"(hlo[0]), "+v"(hhi[0]), "+v"(hlo[1]), "+v"(hhi[1])::"memory");
#pragma unroll
            for (int rd = 0; rd < 2; ++rd) {
                const int task = tid + 512 * rd, c = task / 260, ck = task - 260 * c;
                if (c < nch) {
                    const unsigned r[6] = {hlo[rd].x, hlo[rd].y, hlo[rd].z, hlo[rd].w, hhi[rd].x, hhi[rd].y};
                    unsigned sh[5];
#pragma unroll
                    for (int i = 0; i < 5; ++i) sh[i] = __builtin_amdgcn_alignbyte(r[i + 1], r[i], 2);
                    const lptr dst = lds + c * A2_CH + ck * 16;
                    *(LAS u32x4*)(dst) = (u32x4){sh[0], sh[1], sh[2], sh[3]};
                    *(LAS u32x4*)(dst + A2_HC) = (u32x4){r[1], r[2], r[3], r[4]};
                    *(LAS u32x4*)(dst + 2 * A2_HC) = (u32x4){sh[1], sh[2], sh[3], sh[4]};
                    *(LAS u32x4*)(dst + 3 * A2_HC) = (u32x4){r[2], r[3], r[4], r[5]};
                }
            }
            WAIT_L(0); BAR();
            STAMP(4);
            if (wid < nch) {
                const lptr cb = lds + wid * A2_CH;
                const int i = lane & 15, g = lane >> 4;
                const lptr ab = cb + (i & 3) * A2_HC + ((i >> 2) + g) * 16;
                const lptr bb = cb + A2_U + (g >> 1) * A2_UH + (i + 15) * 32 + (g & 1) * 16;
                f32x4 acc[4][2] = {};
                u32x4 alo[2][4], bq[2][4]; u32x2 aup[2][4];
#define A2LOAD(X, f) do { _Pragma("unroll") for (int c = 0; c < 4; ++c) bq[X][c] = *(LAS u32x4*)(bb + (16 * c - (f)) * 32); \
                    _Pragma("unroll") for (int e = 0; e < 4; ++e) { alo[X][e] = *(LAS u32x4*)(ab + (16 * e + (f)) * 64); aup[X][e] = *(LAS u32x2*)(ab + (16 * e + (f)) * 64 + 16); } SBAR(); } while (0)
#define A2MMA(X) do { _Pragma("unroll") for (int e = 0; e < 4; ++e) { \
                        const u32x6 w6 = {alo[X][e].x, alo[X][e].y, alo[X][e].z, alo[X][e].w, aup[X][e].x, aup[X][e].y}; \
                        const bf16x8 a0 = __builtin_bit_cast(bf16x8, __builtin_shufflevector(w6, w6, 0, 1, 2, 3)), a1 = __builtin_bit_cast(bf16x8, __builtin_shufflevector(w6, w6, 2, 3, 4, 5)); \
                        _Pragma("unroll") for (int c = e; c < 4; ++c) { const bf16x8 bv = __builtin_bit_cast(bf16x8, bq[X][c - e]); acc[c][0] = MFMA16(a0, bv, acc[c][0]); acc[c][1] = MFMA16(a1, bv, acc[c][1]); } } SBAR(); } while (0)
                A2LOAD(0, 0);
                for (int f = 0; f < 16; f += 2) {
                    A2LOAD(1, f + 1);
                    A2MMA(0);
                    A2LOAD(0, (f + 2) & 15);
                    A2MMA(1);
                }
#undef A2LOAD
#undef A2MMA
                const int d = ch0 + wid;
                const float D = bf2f(ssmD[d]);
                u32x4 zw[4];
#pragma unroll
                for (int c = 0; c < 4; ++c) {
                    const u32x4 uu = *(LAS u32x4*)(cb + A2_U + ((3 - g) >> 1) * A2_UH + (16 * c + i + 15) * 32 + ((3 - g) & 1) * 16);
                    float z[8];
#pragma unroll
                    for (int e = 0; e < 8; ++e) { const int ep = 7 - e; const float u = bf2f((uu[ep >> 1] >> (16 * (ep & 1))) & 0xffff);
                        z[e] = rbf(acc[c][e >> 2][e & 3]) + rbf(u * D); }
                    zw[c] = (u32x4){cvtpk(z[0], z[1]), cvtpk(z[2], z[3]), cvtpk(z[4], z[5]), cvtpk(z[6], z[7])};
                }
                asm volatile("s_waitcnt vmcnt(1)" : "+v"(zw[0]), "+v"(zw[1]), "+v"(zw[2]), "+v"(zw[3])::"memory");
                const rsrc_t rZ = mkrsrc(zT + (size_t)d * S, S * 2);
#pragma unroll
                for (int c = 0; c < 4; ++c) __builtin_amdgcn_raw_buffer_store_b128(zw[c], rZ, (unsigned)(32 * (16 * c + i) + 8 * g) * 2u, 0, 0);
            } else WAIT_V(1);
            WAIT_L(0);
        }
        STAMP(5);
        const int row = wid * 32 + r32;
        const int xb = row * 128 + ((hi ^ ((row >> 1) & 7)) << 4);
        const int wb = 32768 + 8 * (lane & 3) + 64 * ((lane >> 2) & 3) + 32 * ((lane >> 4) & 1) + 2048 * hi;
        f32x16 qa[4] = {};
        asm volatile("" : "+v"(qa[0]), "+v"(qa[1]), "+v"(qa[2]), "+v"(qa[3]));
        const int wl = wid * 10 + lane;
        unsigned wo = wid < 7 ? ((lane < 10 && wl < 64) ? (unsigned)(64 * j32 + wl) * 1280u + 512u : 0x80000000u)
                              : (lane < 16 ? (unsigned)(256 + 2 * j32 + (lane >> 3)) * 8192u + (unsigned)(b & 7) * 1024u + (unsigned)(lane & 7) * 128u : 0x80000000u);
        const unsigned wostep = wid < 7 ? 128u : 64u * 8192u;
        const rsrc_t rWarm = wid < 7 ? rX : rW;
        unsigned vx4[4];
#pragma unroll
        for (int p = 0; p < 4; ++p) vx4[p] = vx[p & 1] + p * (8u * 1280u - 1024u);
        bf16x8 wf[16 + PFD], xf[6];
#define QFRAG(j, so_, jj) do { if (((jj) & 3) == 0) xf[(j) >> 2] = ldsfrag(lds + (so_) + (xb ^ (((jj) >> 2) << 5))); \
                      wf[j] = cat8(trread(lds + (so_) + wb + 4096 * ((jj) >> 2) + 512 * ((jj) & 3)), trread(lds + (so_) + wb + 4096 * ((jj) >> 2) + 512 * ((jj) & 3) + 256)); } while (0)
#define QTILE(KIND, PRE, t, st) do { \
            if (KIND == 1 || KIND == 2) WAIT_V(1); \
            BAR(); \
            if (PHOFF) { PHM0(t); PH(0); } \
            const int so = (st) * 49152, son = ((st) == 2 ? 0 : (st) + 1) * 49152; \
            const unsigned sb2 = ldsb + ((st) == 0 ? 2 : (st) - 1) * 49152u;     \
            const unsigned sxt = sx0 + (unsigned)((t) + 2) * 128u, swt = sw0 + (unsigned)((t) + 2) * 64u * 8192u; \
            if (!(PRE)) { _Pragma("unroll") for (int j = 0; j < PFD; ++j) QFRAG(j, so, j); } \
            else { _Pragma("unroll") for (int j = 0; j < PFD; ++j) wf[j] = wf[16 + j]; xf[0] = xf[4]; xf[1] = xf[5]; } \
            SBAR(); \
            _Pragma("unroll") for (int j = 0; j < 16; ++j) { \
                mfma_acc(qa[j & 3], wf[j], xf[j >> 2]); \
                if (j + PFD < 16) QFRAG(j + PFD, so, j + PFD); else if (KIND != 3) QFRAG(j + PFD, son, j + PFD - 16); \
                if (KIND == 0) { if (j < 6) QPIECE(j, 1, ldsb); else if (j < 12) QPIECE(j - 6, 2, ldsb + 49152u); } \
                if (KIND == 1) { \
                    if (j == 0) { setm0(sb2 + wid * 4096u); dma16o<0>(rX, vx4[0], sxt); } \
                    if (j == 1) dma16o<1024>(rX, vx4[1], sxt); if (j == 2) dma16o<2048>(rX, vx4[2], sxt); if (j == 3) dma16o<3072>(rX, vx4[3], sxt); \
                    if (j == 4) { setm0(sb2 + 32768u + wid * 2048u); dma16o<0>(rW, vw, swt); } \
                    if (j == 5) dma16o<1024>(rW, vw, swt - 896u); } \
                if (KIND == 2) { if (j == 0) dma16x2(rK, vlin, wid * 2048u, ldsb + L_K + wid * 2048u); if (j == 2) dma16x2(rK, vlin, 16384u + wid * 2048u, ldsb + L_K + 16384u + wid * 2048u); } \
                if (KIND == 3) { if (j == 0) dma16x2(rV, vlin, wid * 2048u, ldsb + L_V + wid * 2048u); } \
                if (KIND <= 1 && j == 12) { warm_line(dummy, rWarm, wo); wo = (t) < 5 ? wo + wostep : 0x80000000u; } \
                SBAR(); \
            } } while (0)
        PHINIT();
        QTILE(0, false, 0, 2);
        QTILE(1, false, 1, 0);
        { int st = 1;
          for (int t = 2; t < 8; ++t) { QTILE(1, true, t, st); st = st == 2 ? 0 : st + 1; } }
        QTILE(2, true, 8, 1);
        QTILE(3, true, 9, 2);
#undef QTILE
#undef QFRAG
#undef QPIECE
        asm volatile("s_nop 7\n\ts_nop 7" : "+v"(qa[0]), "+v"(qa[1]), "+v"(qa[2]), "+v"(qa[3]));
#pragma unroll
        for (int s = 0; s < 8; ++s) {
            u32x4 w;
#pragma unroll
            for (int e = 0; e < 4; ++e) w[e] = cvtpk(qa[s >> 1][8 * (s & 1) + 2 * e] * QSCALE, qa[s >> 1][8 * (s & 1) + 2 * e + 1] * QSCALE);
            q[s] = __builtin_bit_cast(bf16x8, w);
        }
        WAIT_L(0); BAR();
        if (!grp) {
            const lptr dst = lds + L_PARK + wq4 * 8192 + lane * 16;
#pragma unroll
            for (int s = 0; s < 8; ++s) *(LAS bf16x8*)(dst + s * 1024) = q[s];
        }
        WAIT_L(0);
        WAIT_V(0);
        BAR();
    }
    STAMP(1);
    const int kl = L_K + hi * 1024 + r32 * 16, vl = L_V + 8 * (lane & 3) + 64 * ((lane >> 2) & 3) + 32 * ((lane >> 4) & 1) + 256 * hi;
    const unsigned kdst = ldsb + L_K + wq4 * 4096, vdst = ldsb + L_V + wq4 * 4096;
#define TILE(s_) ((s_) <= x2 + 1 ? (s_) : 2 * (s_) - x2 - 2 + grp)
    f32x16 o[4] = {};
    f32x16 s0, s1, negm = {};
    asm volatile("" : "+v"(negm), "+v"(o[0]), "+v"(o[1]), "+v"(o[2]), "+v"(o[3]));
    float mref = 0.f, lsum = 0.f, lsumA = 0.f;
    bf16x8 fr_[32], pp[4];
    int kb = kl, vb = vl;
    int qrow = (grp ? blkA : blkB) * 128 + wq4 * 32;
    const unsigned kmask = rfl(grp ? (0x7fffu >> x2) << x2 : 0x7fffu), vmask = rfl(grp ? 0xffffu : (0xffffu >> (x2 + 1)) << (x2 + 1)), spmask = rfl(1u | (15u << x2) | 0x10000u);
    unsigned ks1 = 16384u + wq4 * 4096u, ks2 = rfl((unsigned)TILE(2) * 16384u + wq4 * 4096u), ko0 = 0u, ko1 = 16384u, ko2 = rfl(((unsigned)TILE(2) & 3u) * 16384u);
#define ROLL(s_) do { const unsigned t3 = (unsigned)TILE((s_) + 2) << 14; ko0 = ko1; ko1 = ko2; ks1 = ks2; ks2 = t3 + wq4 * 4096u; ko2 = t3 & 0xc000u; \
        asm volatile("" : "+s"(ks1), "+s"(ks2), "+s"(ko0), "+s"(ko1), "+s"(ko2)); } while (0)
#define FRAG(i) ((i) < 16 ? ldsfrag(lds + kb + ((i) >> 1) * 2048 + ((i) & 1) * 512) \
                          : cat8(trread(lds + vb + (((i) - 16) >> 2) * 4096 + ((i) & 3) * 512), trread(lds + vb + (((i) - 16) >> 2) * 4096 + ((i) & 3) * 512 + 2048)))
#define QK(LAST, NEXT) do { _Pragma("unroll") for (int i = 0; i < 16; ++i) { \
            if (i < 2) mfma_init(i ? s1 : s0, fr_[i], q[0], negm); else mfma_acc((i & 1) ? s1 : s0, fr_[i], q[i >> 1]); \
            if (i + PFD < (LAST)) fr_[i + PFD] = FRAG(i + PFD); \
            if ((NEXT) >= 0 && i == 4) ROLL(NEXT); \
            SBAR(); } } while (0)
#define PV() do { _Pragma("unroll") for (int i = 0; i < 16; ++i) { \
            mfma_acc(o[i & 3], fr_[16 + i], pp[i >> 2]); \
            if (i + PFD < 16) fr_[16 + i + PFD] = FRAG(16 + i + PFD); \
            SBAR(); } } while (0)
#define EXPSUM() do { float la = 0.f, lb = 0.f; \
            _Pragma("unroll") for (int ks = 0; ks < 4; ++ks) { u32x4 w; \
                _Pragma("unroll") for (int e = 0; e < 4; ++e) { const int r = 8 * (ks & 1) + 2 * e; \
                    const float ea = __builtin_amdgcn_exp2f((ks >> 1) ? s1[r] : s0[r]), eb = __builtin_amdgcn_exp2f((ks >> 1) ? s1[r + 1] : s0[r + 1]); \
                    la = (ks | e) ? fadd_s(la, ea) : ea; lb = (ks | e) ? fadd_s(lb, eb) : eb; w[e] = cvtpk(ea, eb); } \
                pp[ks] = __builtin_bit_cast(bf16x8, w); } \
            tsum = la + lb; } while (0)
#define ROWMAX(rm) do { float ma = max3f(s0[0], s0[1], s1[0]), mb = max3f(s0[2], s0[3], s1[1]); ma = max3f(ma, s1[2], s1[3]); \
            _Pragma("unroll") for (int r = 4; r < 16; r += 4) { ma = max3f(ma, s0[r], s0[r + 1]); mb = max3f(mb, s0[r + 2], s0[r + 3]); ma = max3f(ma, s1[r], s1[r + 1]); mb = max3f(mb, s1[r + 2], s1[r + 3]); } \
            rm = max2f(ma, mb); \
            auto rr_ = __builtin_amdgcn_permlane32_swap(__float_as_uint(rm), __float_as_uint(rm), false, false); rm = max2f(__uint_as_float(rr_[0]), __uint_as_float(rr_[1])); } while (0)
#define STORE_A() do { const lptr tb = lds + L_PARK + wq4 * 8192; \
            char* cb = ctxf + ((size_t)(blkA * 8 + wq4 * 2) * 128 + head * 4) * 1024 + lane * 16; \
            _Pragma("unroll") for (int i = 0; i < 8; ++i) { const int rr = 16 * (i >> 2) + (lane & 15), ch = 4 * (i & 3) + (lane >> 4); \
                *(u32x4*)(cb + (size_t)(i >> 2) * 131072 + (i & 3) * 1024) = *(LAS u32x4*)(tb + rr * 256 + ((ch ^ (rr & 15)) << 4)); } } while (0)
#define VPHASE(LASTT) do { \
            if ((kmask >> s) & 1u) dma16x4(rK, vlin, ks2, kdst + ko2); \
            if ((vmask >> s) & 1u) dma16x4(rV, vlin, ks1, vdst + ko1); \
            const bool sp = (spmask >> s) & 1u; \
            if (__builtin_expect(sp, 0)) { \
                asm volatile("s_nop 7\n\ts_nop 7" : "+v"(o[0]), "+v"(o[1]), "+v"(o[2]), "+v"(o[3]), "+v"(s0), "+v"(s1)); \
                bool first = s == 0; \
                if (grp && s == x2 + 2) {     \
                    const float rl = __builtin_amdgcn_rcpf(lsumA); \
                    const lptr tb = lds + L_PARK + wq4 * 8192; \
                    _Pragma("unroll") for (int c = 0; c < 8; ++c) { const int d = c >> 1, r = (c & 1) * 8; u32x4 w; \
                        w.x = cvtpk(o[d][r] * rl, o[d][r + 1] * rl); w.y = cvtpk(o[d][r + 2] * rl, o[d][r + 3] * rl); w.z = cvtpk(o[d][r + 4] * rl, o[d][r + 5] * rl); w.w = cvtpk(o[d][r + 6] * rl, o[d][r + 7] * rl); \
                        *(LAS u32x4*)(tb + r32 * 256 + (((2 * c + hi) ^ (r32 & 15)) << 4)) = w; } \
                    _Pragma("unroll") for (int d = 0; d < 4; ++d) o[d] = (f32x16){}; \
                    first = true; \
                } \
                if (grp && s == x2 + 3) STORE_A(); \
                const int rel = qrow - 64 * TILE(s);     \
                if (rel <= 32) { const int th = r32 - 4 * hi; \
                    if (rel == 32) { _Pragma("unroll") for (int r = 0; r < 16; ++r) if ((r & 3) + 8 * (r >> 2) > th) s1[r] = -INFINITY; } \
                    else { if (rel == 0) { _Pragma("unroll") for (int r = 0; r < 16; ++r) if ((r & 3) + 8 * (r >> 2) > th) s0[r] = -INFINITY; } \
                           else { _Pragma("unroll") for (int r = 0; r < 16; ++r) s0[r] = -INFINITY; } \
                           _Pragma("unroll") for (int r = 0; r < 16; ++r) s1[r] = -INFINITY; } } \
                if (first) {     \
                    float rm; ROWMAX(rm); \
                    mref = __builtin_fmaxf(rm, -1e30f); \
                    _Pragma("unroll") for (int r = 0; r < 16; ++r) { s0[r] -= mref; s1[r] -= mref; } \
                    bcast16(negm, hi ? 0.f : 1.f, -mref); \
                } \
            } \
            float tsum; \
            EXPSUM(); \
            if (__builtin_expect(__any(!(tsum <= SUMTHR)), 0)) {     \
                float rm; ROWMAX(rm); \
                const float dl = __builtin_fmaxf(rm, 0.f), f = __builtin_amdgcn_exp2f(-dl); \
                mref += dl; lsum *= f; \
                _Pragma("unroll") for (int r = 0; r < 16; ++r) { s0[r] -= dl; s1[r] -= dl; } \
                bcast16(negm, hi ? 0.f : 1.f, -mref); \
                _Pragma("unroll") for (int d = 0; d < 4; ++d) _Pragma("unroll") for (int r = 0; r < 16; ++r) o[d][r] *= f; \
                EXPSUM(); \
            } \
            lsum += tsum; \
            if (__builtin_expect(sp, 0)) { if (grp && s == x2 + 1) {     \
                auto rr = __builtin_amdgcn_permlane32_swap(__float_as_uint(lsum), __float_as_uint(lsum), false, false); lsumA = __uint_as_float(rr[0]) + __uint_as_float(rr[1]); \
                _Pragma("unroll") for (int u = 0; u < 8; ++u) q[u] = ldsfrag(lds + L_PARK + wq4 * 8192 + lane * 16 + u * 1024); \
                bcast16(negm, 0.f, 0.f); mref = 0.f; lsum = 0.f; qrow = blkB * 128 + wq4 * 32; \
                WAIT_L(0); } } \
            asm volatile("" : "+v"(pp[0]), "+v"(pp[1]), "+v"(pp[2]), "+v"(pp[3]), "+v"(lsum)); \
            SBAR(); \
            vb = vl + ko0; kb = kl + ko1; \
            _Pragma("unroll") for (int u = 0; u < PFD; ++u) fr_[((LASTT) ? 16 : 0) + u] = FRAG(((LASTT) ? 16 : 0) + u); \
            } while (0)
    if (grp) BAR();
    {
#pragma unroll
        for (int u = 0; u < PFD; ++u) fr_[u] = FRAG(u);
        SBAR();
        __builtin_amdgcn_s_setprio(MPRIO);
        QK(16, -1);
        __builtin_amdgcn_s_setprio(0);
    }
    int s = 0;
    PHINIT();
    for (; s < 16; ++s) {
        WAIT_V(0); BAR();
        if (!PHOFF) { PHM0(4 * s + 0); PH(0); }
        VPHASE(false);
        if (!PHOFF) { PHM0(4 * s + 1); PH(1); }
        BAR();
        if (!PHOFF) { PHM0(4 * s + 2); PH(2); }
        __builtin_amdgcn_s_setprio(MPRIO);
        QK(32, s + 1);
        PV();
        __builtin_amdgcn_s_setprio(0);
        if (!PHOFF) { PHM0(4 * s + 3); PH(3); }
    }
    WAIT_V(0); BAR();
    VPHASE(true);
    BAR();
    __builtin_amdgcn_s_setprio(MPRIO);
    PV();
    __builtin_amdgcn_s_setprio(0);
    asm volatile("s_nop 7\n\ts_nop 7" : "+v"(o[0]), "+v"(o[1]), "+v"(o[2]), "+v"(o[3]));
    if (grp && x2 + 3 > 16) { WAIT_L(0); STORE_A(); }
#undef FRAG
#undef VPHASE
#undef EXPSUM
#undef ROWMAX
#undef STORE_A
#undef QK
#undef PV
#undef ROLL
#undef TILE
    STAMP(2);
    { auto rr = __builtin_amdgcn_permlane32_swap(__float_as_uint(lsum), __float_as_uint(lsum), false, false); lsum = __uint_as_float(rr[0]) + __uint_as_float(rr[1]); }
    if (!grp) BAR();
    BAR();
    LAS f32x2* ml = (LAS f32x2*)(lds + 65536);
    if (hi == 0) ml[wid * 32 + r32] = (f32x2){mref, lsum};
#define MERGE(KEEP, SEND) do { \
    { const lptr dst = lds + wid * 8192 + lane * 16; \
      _Pragma("unroll") for (int e = 0; e < 8; ++e) { const int d = SEND + (e >> 2), r = (e & 3) * 4; \
          *(LAS f32x4*)(dst + e * 1024) = (f32x4){o[d][r], o[d][r + 1], o[d][r + 2], o[d][r + 3]}; } } \
    WAIT_L(0); BAR(); \
    const f32x2 oth = ml[(wid ^ 4) * 32 + r32]; \
    const float mm = __builtin_fmaxf(mref, oth.x), w0 = __builtin_amdgcn_exp2f(mref - mm), w1 = __builtin_amdgcn_exp2f(oth.x - mm); \
    const float rl = __builtin_amdgcn_rcpf(lsum * w0 + oth.y * w1), sc0 = w0 * rl, sc1 = w1 * rl; \
    { const lptr src = lds + (wid ^ 4) * 8192 + lane * 16; \
      u32x4 w[4]; \
      _Pragma("unroll") for (int c = 0; c < 4; ++c) {     \
          const int d = KEEP + (c >> 1), r = (c & 1) * 8; \
          const f32x4 a0 = *(LAS f32x4*)(src + (2 * c) * 1024), a1 = *(LAS f32x4*)(src + (2 * c + 1) * 1024); \
          w[c].x = cvtpk(o[d][r] * sc0 + a0[0] * sc1, o[d][r + 1] * sc0 + a0[1] * sc1); w[c].y = cvtpk(o[d][r + 2] * sc0 + a0[2] * sc1, o[d][r + 3] * sc0 + a0[3] * sc1); \
          w[c].z = cvtpk(o[d][r + 4] * sc0 + a1[0] * sc1, o[d][r + 5] * sc0 + a1[1] * sc1); w[c].w = cvtpk(o[d][r + 6] * sc0 + a1[2] * sc1, o[d][r + 7] * sc0 + a1[3] * sc1); } \
        \
      WAIT_L(0); \
      const lptr tb = lds + (wid ^ 4) * 8192; \
      _Pragma("unroll") for (int c = 0; c < 4; ++c) *(LAS u32x4*)(tb + r32 * 128 + (((2 * c + hi) ^ (r32 & 7)) << 4)) = w[c]; \
      WAIT_L(0); \
      char* cb = ctxf + ((size_t)(blkB * 8 + wq4 * 2) * 128 + head * 4 + KEEP) * 1024 + lane * 16; \
      _Pragma("unroll") for (int i = 0; i < 4; ++i) { const int rr = 16 * (i >> 1) + (lane & 15), ch = 4 * (i & 1) + (lane >> 4); \
          *(u32x4*)(cb + (size_t)(i >> 1) * 131072 + (i & 1) * 1024) = *(LAS u32x4*)(tb + rr * 128 + ((ch ^ (rr & 7)) << 4)); } } } while (0)
    if (grp) MERGE(2, 0); else MERGE(0, 2);
#undef MERGE
    STAMP(3);
    STAMP_OUT();
}

constexpr int K3_T2 = 140 * 1024, K3_LDS = K3_T2 + 64 * 176;
__global__ __launch_bounds__(512, 2) void k3_oproj(const char* __restrict__ ctxf, const char* __restrict__ wt, const u16* __restrict__ x, const u16* __restrict__ zT,
                                                   const float* __restrict__ gate, u16* __restrict__ out DBGARG) {
    STAMP_DECL; STAMP(0);
    extern __shared__ __attribute__((aligned(16))) char smem[];
    const lptr lds = (lptr)smem;
    const int tid = threadIdx.x, lane = tid & 63, fr = lane & 15, fq = lane >> 4;
    const int wid = rfl(tid >> 6);
    const int b = blockIdx.x, xcd = b & 7, j32 = b >> 3, pm = 8 * (xcd >> 1) + (j32 & 7), pn = 4 * (xcd & 1) + (j32 >> 3);
    const rsrc_t rA = mkrsrc(ctxf, S * 4096 * 2), rB = mkrsrc(wt, 4096 * DM * 2);
    const unsigned vlin = (unsigned)lane * 16u, sa = (unsigned)((4 * pm) * 128 + 16 * wid) * 1024u, sb = (unsigned)((16 * wid) * 40 + 5 * pn) * 1024u;
    f32x4 acc[4][5] = {};
    bf16x8 af[3][4], bfr[3][5];
#define LOADF(X, s_, i) do { if ((i) < 4) af[X][(i) & 3] = __builtin_bit_cast(bf16x8, __builtin_amdgcn_raw_buffer_load_b128(rA, vlin, sa + ((i) & 3) * 131072u + (s_) * 1024u, 0)); \
                             else bfr[X][((i) - 4) % 5] = __builtin_bit_cast(bf16x8, __builtin_amdgcn_raw_buffer_load_b128(rB, vlin + (((i) - 4) % 5) * 1024u, sb + (s_) * 40960u, 0)); } while (0)
#pragma unroll
    for (int s = 0; s < 2; ++s)
#pragma unroll
        for (int i = 0; i < 9; ++i) LOADF(s, s, (i == 0 ? 4 : (i <= 4 ? i - 1 : i)));
    const int t0 = pm * 64, c0 = pn * 80;
    u32x4 zr[2]; u32x2 xq[3]; f32x2 gv[3];
#pragma unroll
    for (int r = 0; r < 2; ++r) { const int i = tid + 512 * r; zr[r] = i < 640 ? *(const u32x4*)(zT + (size_t)(c0 + (i >> 3)) * S + t0 + (i & 7) * 8) : (u32x4){0, 0, 0, 0}; }
#pragma unroll
    for (int u = 0; u < 3; ++u) { const int e = (wid + 8 * u) < 20 ? wid + 8 * u : wid;
        xq[u] = *(const u32x2*)(x + (size_t)(t0 + (e & 3) * 16 + fr) * DM + c0 + (e >> 2) * 16 + fq * 4); gv[u] = *(const f32x2*)(gate + (size_t)(t0 + (e & 3) * 16 + fr) * 2); }
    SBAR();
#pragma unroll
    for (int s = 0; s < 16; ++s) {
#pragma unroll
        for (int j = 0; j < 20; ++j) {
            acc[j & 3][j >> 2] = MFMA16(bfr[s % 3][j >> 2], af[s % 3][j & 3], acc[j & 3][j >> 2]);
            if (s + 2 < 16 && (j & 1) == 0 && j < 18) { const int i = j >> 1; LOADF((s + 2) % 3, s + 2, (i == 0 ? 4 : (i <= 4 ? i - 1 : i))); }
            SBAR();
        }
        if (s == 1) STAMP(1);
    }
#undef LOADF
    STAMP(2);
#pragma unroll
    for (int r = 0; r < 2; ++r) { const int i = tid + 512 * r;
        if (i < 640) {
#pragma unroll
            for (int e = 0; e < 8; ++e) *(LAS u16*)(lds + K3_T2 + ((i & 7) * 8 + e) * 176 + (i >> 3) * 2) = (u16)((zr[r][e >> 1] >> (16 * (e & 1))) & 0xffff); } }
#pragma unroll
    for (int e = 0; e < 20; ++e) if ((e & 7) != wid) *(LAS f32x4*)(lds + (e * 7 + (wid < (e & 7) ? wid : wid - 1)) * 1024 + lane * 16) = acc[e & 3][e >> 2];
    f32x4 own[3];
#define OWN(W) case W: own[0] = acc[(W) & 3][(W) >> 2]; own[1] = acc[((W) + 8) & 3][((W) + 8) >> 2]; own[2] = acc[(((W) & 3) + 16) & 3][(((W) & 3) + 16) >> 2]; break;
    switch (wid) { OWN(0) OWN(1) OWN(2) OWN(3) OWN(4) OWN(5) OWN(6) default: OWN(7) }
#undef OWN
    WAIT_L(0); BAR();
    STAMP(3);
#pragma unroll
    for (int u = 0; u < 3; ++u) {
        const int e = wid + 8 * u;
        if (e < 20) {
            const lptr src = lds + e * 7 * 1024 + lane * 16;
            f32x4 p[8];
            f32x4 sum = {0.f, 0.f, 0.f, 0.f};
#pragma unroll
            for (int r = 0; r < 7; ++r) p[r] = *(LAS f32x4*)(src + r * 1024);
#pragma unroll
            for (int w = 0; w < 8; ++w) sum += w == wid ? own[u] : (w < wid ? p[w < 7 ? w : 6] : p[w > 0 ? w - 1 : 0]);
            const int row = (e & 3) * 16 + fr, col = (e >> 2) * 16 + fq * 4;
            const u32x2 zz = *(LAS u32x2*)(lds + K3_T2 + row * 176 + col * 2);
            float o[4];
#pragma unroll
            for (int j = 0; j < 4; ++j) {
                const float a = rbf(bf2f((xq[u][j >> 1] >> (16 * (j & 1))) & 0xffff) * bf2f((zz[j >> 1] >> (16 * (j & 1))) & 0xffff));
                o[j] = rbf(gv[u].x * a) + rbf(gv[u].y * rbf(sum[j]));
            }
            *(u32x2*)(out + (size_t)(t0 + row) * DM + c0 + col) = (u32x2){cvtpk(o[0], o[1]), cvtpk(o[2], o[3])};
        }
    }
    STAMP(4); STAMP_OUT();
}

extern "C" void kernel_launch(void* const* d_in, const int* in_sizes, int n_in, void* d_out, int out_size, void* d_ws, size_t ws_size, hipStream_t stream) {
    (void)in_sizes; (void)n_in; (void)out_size; (void)ws_size;
    const u16* Aq = (const u16*)d_in[0]; const u16* Ak = (const u16*)d_in[1]; const u16* Av = (const u16*)d_in[2];
    const u16* shk = (const u16*)d_in[3]; const u16* ssmk = (const u16*)d_in[4]; const u16* ssmD = (const u16*)d_in[5];
    const u16* ckv = (const u16*)d_in[7]; const u16* wq = (const u16*)d_in[8]; const u16* wuk = (const u16*)d_in[9];
    const u16* wuv = (const u16*)d_in[10]; const u16* wo = (const u16*)d_in[11]; const u16* gw = (const u16*)d_in[12];
    char* ws = (char*)d_ws;
    (void)hipFuncSetAttribute((const void*)k1_kvproj, hipFuncAttributeMaxDynamicSharedMemorySize, K1_LDS);
    (void)hipFuncSetAttribute((const void*)k2_attn, hipFuncAttributeMaxDynamicSharedMemorySize, K2_LDS);
    (void)hipFuncSetAttribute((const void*)k3_oproj, hipFuncAttributeMaxDynamicSharedMemorySize, K3_LDS);
    k1_kvproj<<<256, 512, K1_LDS, stream>>>(ckv, wuk, wuv, ws + WS_K, ws + WS_V, Ak, Av, shk, (u16*)(ws + WS_UR), Aq, gw, (float*)(ws + WS_GATE), wo, ws + WS_WT DBGPASS(0));
    k2_attn<<<256, 512, K2_LDS, stream>>>(Aq, wq, ws + WS_K, ws + WS_V, ws + WS_CTX, (const u16*)(ws + WS_UR), ssmk, ssmD, (u16*)(ws + WS_ZT) DBGPASS(1));
    k3_oproj<<<256, 512, K3_LDS, stream>>>(ws + WS_CTX, ws + WS_WT, Aq, (const u16*)(ws + WS_ZT), (const float*)(ws + WS_GATE), (u16*)d_out DBGPASS(2));
}
```

```cpp
#include <hip/hip_runtime.h>
#include <stdint.h>

#define DI __device__ __forceinline__
#define LAS __attribute__((address_space(3)))
typedef unsigned short u16;
typedef __bf16 bf16x8 __attribute__((ext_vector_type(8)));
typedef __bf16 bf16x2 __attribute__((ext_vector_type(2)));
typedef short s16x4 __attribute__((ext_vector_type(4)));
typedef short s16x8 __attribute__((ext_vector_type(8)));
typedef float f32x2 __attribute__((ext_vector_type(2)));
typedef float f32x4 __attribute__((ext_vector_type(4)));
typedef float f32x16 __attribute__((ext_vector_type(16)));
typedef unsigned u32x4 __attribute__((ext_vector_type(4)));
typedef unsigned u32x2 __attribute__((ext_vector_type(2)));
typedef __amdgpu_buffer_rsrc_t rsrc_t;
typedef LAS char* lptr;

constexpr int S = 2048, DM = 640, NH = 32, HD = 128, DC = 512;
constexpr size_t MB = 1u << 20;
constexpr size_t WS_K = 0, WS_V = 16 * MB, WS_CTX = 32 * MB, WS_WT = 48 * MB, WS_UR = 72 * MB, WS_ZT = 76 * MB, WS_GATE = 80 * MB;

#define SBAR() __builtin_amdgcn_sched_barrier(0)
#define WAIT_V(n) asm volatile("s_waitcnt vmcnt(" #n ")" ::: "memory")
#define WAIT_L(n) asm volatile("s_waitcnt lgkmcnt(" #n ")" ::: "memory")
#define BAR() asm volatile("s_barrier" ::: "memory")
#define MFMA32(a, b, c) __builtin_amdgcn_mfma_f32_32x32x16_bf16((a), (b), (c), 0, 0, 0)
#define MFMA16(a, b, c) __builtin_amdgcn_mfma_f32_16x16x32_bf16((a), (b), (c), 0, 0, 0)

DI unsigned rfl(unsigned x) { return __builtin_amdgcn_readfirstlane(x); }
DI unsigned cvtpk(float lo, float hi) { f32x2 v = {lo, hi}; bf16x2 b = __builtin_convertvector(v, bf16x2); return __builtin_bit_cast(unsigned, b); }
DI float bf2f(unsigned b16) { return __uint_as_float(b16 << 16); }
DI float rbf(float x) { return (float)(__bf16)x; }
DI rsrc_t mkrsrc(const void* p, unsigned bytes) { return __builtin_amdgcn_make_buffer_rsrc((void*)p, (short)0, (int)bytes, 0x00020000); }
DI void dma16(rsrc_t r, unsigned voff, unsigned soff, unsigned lds) {
    asm volatile("s_mov_b32 m0, %0\n\ts_nop 0\n\tbuffer_load_dwordx4 %1, %2, %3 offen lds" ::"s"(lds), "v"(voff), "s"(r), "s"(soff) : "memory");
}
DI void dma16x4(rsrc_t r, unsigned voff, unsigned soff, unsigned lds) {
    asm volatile("s_mov_b32 m0, %0\n\ts_nop 0\n\tbuffer_load_dwordx4 %1, %2, %3 offen lds\n\tbuffer_load_dwordx4 %1, %2, %3 offen offset:1024 lds\n\t"
                 "buffer_load_dwordx4 %1, %2, %3 offen offset:2048 lds\n\tbuffer_load_dwordx4 %1, %2, %3 offen offset:3072 lds" ::"s"(lds), "v"(voff), "s"(r), "s"(soff) : "memory");
}
template <int P, int BIT> DI void dma16p(rsrc_t r, unsigned voff, unsigned soff, unsigned lds, unsigned en) {
    asm volatile("s_bitcmp1_b32 %4, %6\n\ts_cbranch_scc0 1f\n\ts_mov_b32 m0, %0\n\ts_nop 0\n\tbuffer_load_dwordx4 %1, %2, %3 offen offset:%5 lds\n1:" ::"s"(lds), "v"(voff), "s"(r), "s"(soff), "s"(en), "i"(P * 1024), "i"(BIT) : "memory", "scc");
}
DI void dma16x2(rsrc_t r, unsigned voff, unsigned soff, unsigned lds) {
    asm volatile("s_mov_b32 m0, %0\n\ts_nop 0\n\tbuffer_load_dwordx4 %1, %2, %3 offen lds\n\tbuffer_load_dwordx4 %1, %2, %3 offen offset:1024 lds" ::"s"(lds), "v"(voff), "s"(r), "s"(soff) : "memory");
}
DI void setm0(unsigned lds) { asm volatile("s_mov_b32 m0, %0\n\ts_nop 0" ::"s"(lds) : "memory"); }
template <int OFF> DI void dma16o(rsrc_t r, unsigned voff, unsigned soff) { asm volatile("buffer_load_dwordx4 %0, %1, %2 offen offset:%3 lds" ::"v"(voff), "s"(r), "s"(soff), "i"(OFF) : "memory"); }
DI bf16x8 ldsfrag(lptr p) { return *(LAS bf16x8*)p; }
typedef short v4i16_t __attribute__((ext_vector_type(4)));
DI s16x4 trread(lptr p) { return __builtin_amdgcn_ds_read_tr16_b64_v4i16((LAS v4i16_t*)p); }
DI bf16x8 cat8(s16x4 lo, s16x4 hi) { return __builtin_bit_cast(bf16x8, __builtin_shufflevector(lo, hi, 0, 1, 2, 3, 4, 5, 6, 7)); }

#define PH(k) do {} while (0)
#define PHM0(idx) do {} while (0)
#define PHINIT() do {} while (0)
#define STAMP_DECL do {} while (0)
#define STAMP(k) do {} while (0)
#define STAMP_OUT() do {} while (0)
#define DBGARG
#define DBGPASS(off)
DI int lds_byte(int r, int c) { const int st = (r >> 4) * 2 + (c >> 5), rr = r & 15, cc = c & 31, ob = rr * 64 + cc * 2; return st * 1024 + (ob ^ (((ob >> 9) & 1) << 5)); }
DI void stage_rc(int b, int& R, int& C) { const int st = b / 1024, sb = b % 1024, swz = sb ^ (((sb >> 9) & 1) << 5); R = (st >> 1) * 16 + swz / 64; C = (st & 1) * 32 + (swz % 64) / 2; }

#define EXP_K1 0
constexpr int K1_SIDE = 8 * 16384, K1_LDS = K1_SIDE + 23552;
DI void ld16a(u32x4& d, rsrc_t r, unsigned voff) { asm volatile("buffer_load_dwordx4 %0, %1, %2, 0 offen" : "=v"(d) : "v"(voff), "s"(r) : "memory"); }
__global__ __launch_bounds__(512, 2) void k1_kvproj(const u16* __restrict__ ckv, const u16* __restrict__ wuk, const u16* __restrict__ wuv, char* __restrict__ Kout, char* __restrict__ Vout,
                                                    const u16* __restrict__ Ak, const u16* __restrict__ Av, const u16* __restrict__ shk, u16* __restrict__ ur,
                                                    const u16* __restrict__ x, const u16* __restrict__ gw, float* __restrict__ gate,
                                                    const u16* __restrict__ wo, char* __restrict__ wt DBGARG) {
    STAMP_DECL; STAMP(0);
    extern __shared__ __attribute__((aligned(16))) char smem[];
    const lptr lds = (lptr)smem;
    const unsigned ldsb = (unsigned)(uintptr_t)smem;
    const int tid = threadIdx.x, lane = tid & 63, fr = lane & 15, fq = lane >> 4;
    const int wid = rfl(tid >> 6), wr = wid >> 2, wc = wid & 3;
    const int b = blockIdx.x, head = 4 * (b & 7) + (b >> 6), pm = (b >> 3) & 7;
    const rsrc_t rA = mkrsrc(ckv, S * DC * 2), rK = mkrsrc(wuk, DC * 4096 * 2), rV = mkrsrc(wuv, DC * 4096 * 2);
    unsigned voffA, voffB;
    { int R, C; stage_rc(tid * 16, R, C); voffA = (unsigned)(R * DC + C) * 2u;
      const int row = 4 * wid + (lane >> 4), swz = ((row & 3) << 2) | (((row >> 2) & 1) << 1) | ((row >> 3) & 1);
      voffB = (unsigned)row * 8192u + (unsigned)(((lane & 15) ^ swz) * 16); }
    const unsigned sA0 = (EXP_K1 ? 0u : (unsigned)pm * 256u * 1024u), sB0 = (EXP_K1 ? 0u : (unsigned)head * 256u), ldw = ldsb + (unsigned)wid * 1024u;
#define SA(b_, h_) (((b_) * 2 + (h_)) * 16384)
#define SB(b_, h_) ((4 + (b_) * 2 + (h_)) * 16384)
#define STAGE_A(b_, h_, kt) do { dma16(rA, voffA, sA0 + (h_) * 131072u + (unsigned)(kt) * 128u, ldw + SA(b_, h_)); dma16(rA, voffA, sA0 + (h_) * 131072u + (unsigned)(kt) * 128u + 65536u, ldw + SA(b_, h_) + 8192); } while (0)
#define STAGE_B(b_, h_, kt) do { dma16((h_) ? rV : rK, voffB, sB0 + (unsigned)(kt) * 524288u, ldw + SB(b_, h_)); dma16((h_) ? rV : rK, voffB, sB0 + (unsigned)(kt) * 524288u + 262144u, ldw + SB(b_, h_) + 8192); } while (0)
    const int aoff = lds_byte(wr * 64 + fr, fq * 8);
    int boff[2][2];
    { const int g = lane >> 4, q = (lane >> 2) & 3, p = lane & 3;
      const int base = (8 * g + q) * 256 + ((wc ^ q) << 6) + ((p >> 1) << 5) + ((g & 1) << 4) + 8 * (p & 1);
#pragma unroll
      for (int n = 0; n < 2; ++n)
#pragma unroll
          for (int t = 0; t < 2; ++t) boff[n][t] = (base ^ (t << 5) ^ (n << 4)) + t * 1024; }
#define LDA(dst, b_, h_) do { _Pragma("unroll") for (int m = 0; m < 4; ++m) _Pragma("unroll") for (int k = 0; k < 2; ++k) dst[m][k] = ldsfrag(lds + SA(b_, h_) + aoff + m * 2048 + k * 1024); } while (0)
#define LDB(dst, b_, h_) do { _Pragma("unroll") for (int n = 0; n < 2; ++n) _Pragma("unroll") for (int k = 0; k < 2; ++k) \
        dst[n][k] = cat8(trread(lds + SB(b_, h_) + boff[n][0] + k * 8192), trread(lds + SB(b_, h_) + boff[n][1] + k * 8192)); } while (0)
#define MMA(ai, bj, At_, Bt_) do { __builtin_amdgcn_s_setprio(1); _Pragma("unroll") for (int m = 0; m < 4; ++m) _Pragma("unroll") for (int n = 0; n < 2; ++n) _Pragma("unroll") for (int k = 0; k < 2; ++k) \
        acc[ai][bj][m][n] = MFMA16(Bt_[n][k], At_[m][k], acc[ai][bj][m][n]); __builtin_amdgcn_s_setprio(0); } while (0)
    f32x4 acc[2][2][4][2] = {};
    bf16x8 At[4][2], B0[2][2], B1[2][2];
    constexpr int nt = DC / 64;
    STAGE_B(0, 0, 0); STAGE_A(0, 0, 0); STAGE_B(0, 1, 0); STAGE_A(0, 1, 0);
    {
        const int t0 = (b >> 4) * 128, d0 = (b & 15) * 40;
        const rsrc_t rk = mkrsrc(Ak, S * DM * 2), rv = mkrsrc(Av, S * DM * 2), rs = mkrsrc(shk, DM * 8);
#pragma unroll
        for (int j = 0; j < 3; ++j) { const int pc = wid + 8 * j;
            if (pc < 11) { const int idx = pc * 64 + lane, row = idx / 5, t = t0 - 3 + row; dma16(rk, (t >= 0 && row < 131) ? (unsigned)(t * DM + d0 + (idx % 5) * 8) * 2u : 0x80000000u, 0u, ldsb + K1_SIDE + pc * 1024u); }
            else if (pc < 21) { const int idx = (pc - 11) * 64 + lane; dma16(rv, (unsigned)((t0 + idx / 5) * DM + d0 + (idx % 5) * 8) * 2u, 0u, ldsb + K1_SIDE + pc * 1024u); }
            else dma16(rs, (pc == 21 && lane < 20) ? (unsigned)(d0 * 8 + lane * 16) : 0x80000000u, 0u, ldsb + K1_SIDE + (pc == 21 ? 21504u : 22528u)); }
    }
    if (wr == 1) BAR();
    WAIT_V(7); BAR();
    STAGE_B(1, 0, 1); STAGE_A(1, 0, 1); STAGE_B(1, 1, 1);
    WAIT_V(6); BAR();
    STAMP(1);
    for (int t = 0; t < nt - 2; t += 2) {
        LDB(B0, 0, 0); SBAR(); LDA(At, 0, 0); STAGE_A(1, 1, t + 1);
        WAIT_L(8); BAR(); WAIT_L(0); MMA(0, 0, At, B0); BAR(); SBAR();
        LDB(B1, 0, 1); STAGE_B(0, 0, t + 2);
        BAR(); WAIT_L(0); MMA(0, 1, At, B1); BAR();
        LDA(At, 0, 1); STAGE_A(0, 0, t + 2);
        BAR(); WAIT_L(0); MMA(1, 0, At, B0); BAR(); SBAR();
        STAGE_B(0, 1, t + 2);
        WAIT_V(6); BAR(); MMA(1, 1, At, B1); BAR();
        LDB(B0, 1, 0); SBAR(); LDA(At, 1, 0); STAGE_A(0, 1, t + 2);
        WAIT_L(8); BAR(); WAIT_L(0); MMA(0, 0, At, B0); BAR(); SBAR();
        LDB(B1, 1, 1); STAGE_B(1, 0, t + 3);
        BAR(); WAIT_L(0); MMA(0, 1, At, B1); BAR();
        LDA(At, 1, 1); STAGE_A(1, 0, t + 3);
        BAR(); WAIT_L(0); MMA(1, 0, At, B0); BAR(); SBAR();
        STAGE_B(1, 1, t + 3);
        WAIT_V(6); BAR(); MMA(1, 1, At, B1); BAR();
    }
    { LDB(B0, 0, 0); WAIT_V(0); LDA(At, 0, 0); STAGE_A(1, 1, nt - 1);
      BAR(); WAIT_L(0); MMA(0, 0, At, B0); BAR();
      LDB(B1, 0, 1); BAR(); WAIT_L(0); MMA(0, 1, At, B1); BAR();
      LDA(At, 0, 1); WAIT_V(4); BAR(); WAIT_L(0); MMA(1, 0, At, B0); MMA(1, 1, At, B1); BAR(); }
    { LDB(B0, 1, 0); LDA(At, 1, 0); WAIT_V(2); BAR(); WAIT_L(0); MMA(0, 0, At, B0); BAR();
      LDB(B1, 1, 1); WAIT_V(0); BAR(); WAIT_L(0); MMA(0, 1, At, B1); BAR();
      LDA(At, 1, 1); BAR(); WAIT_L(0); MMA(1, 0, At, B0); MMA(1, 1, At, B1); BAR(); }
    if (wr == 0) BAR();
    STAMP(2);
    {
        const rsrc_t rwo = mkrsrc(wo, 4096 * DM * 2), rx = mkrsrc(x, S * DM * 2), rg = mkrsrc(gw, DM * 2);
#pragma unroll
        for (int j = 0; j < 3; ++j) { const int pc = wid + 8 * j, idx = pc * 64 + lane, row = idx / 40, ch = idx % 40;
            dma16(rwo, pc < 20 ? (unsigned)((b >> 1) * 32 + row) * 1280u + (unsigned)(b & 1) * 640u + (unsigned)ch * 16u : 0x80000000u, 0u, ldsb + (pc < 20 ? pc : 20) * 1024u); }
        u32x4 xg[2], gg[2];
#pragma unroll
        for (int j = 0; j < 2; ++j) { const int ch = 64 * j + lane;
            ld16a(xg[j], rx, ch < 80 ? (unsigned)((8 * b + wid) * DM + ch * 8) * 2u : 0x80000000u); ld16a(gg[j], rg, ch < 80 ? (unsigned)ch * 16u : 0x80000000u); }
        STAMP(4);
        const int t0 = (b >> 4) * 128, d0 = (b & 15) * 40, it = wid * 40 + lane, c5 = it % 5, tp = it / 5;
        const lptr T = lds + 32768;
#define K1_EPI() do { \
        char* Kh = Kout + (size_t)head * 524288; char* Vh = Vout + (size_t)head * 524288; \
_Pragma("unroll") \
        for (int ai = 0; ai < 2; ++ai) { \
            const int tile = 4 * pm + 2 * ai + wr; \
_Pragma("unroll") \
            for (int m = 0; m < 4; ++m) { \
                const int kk = 16 * m + fr; \
_Pragma("unroll") \
                for (int bj = 0; bj < 2; ++bj) { \
                    const f32x4 v0 = acc[ai][bj][m][0], v1 = acc[ai][bj][m][1]; \
                    u32x4 w; w.x = cvtpk(v0[0], v0[1]); w.y = cvtpk(v0[2], v0[3]); w.z = cvtpk(v1[0], v1[1]); w.w = cvtpk(v1[2], v1[3]); \
                    if (bj == 0) *(u32x4*)(Kh + tile * 16384 + (4 * wc + fq) * 1024 + kk * 16) = w; \
                    else *(u32x4*)(Vh + tile * 16384 + ((kk >> 3) * 4 + wc) * 512 + (kk & 7) * 64 + fq * 16) = w; \
                } \
            } \
        } \
        } while (0)
#define K1_A1() do { \
        if (lane < 40) { \
            u32x4 kr[5], vr[2], sw[4]; \
_Pragma("unroll") \
            for (int j = 0; j < 5; ++j) kr[j] = *(LAS u32x4*)(lds + K1_SIDE + (2 * tp + j) * 80 + c5 * 16); \
_Pragma("unroll") \
            for (int j = 0; j < 2; ++j) vr[j] = *(LAS u32x4*)(lds + K1_SIDE + 11264 + (2 * tp + j) * 80 + c5 * 16); \
_Pragma("unroll") \
            for (int j = 0; j < 4; ++j) sw[j] = *(LAS u32x4*)(lds + K1_SIDE + 21504 + c5 * 64 + j * 16); \
_Pragma("unroll") \
            for (int e = 0; e < 8; ++e) { \
                const unsigned s01 = sw[e >> 1][2 * (e & 1)], s23 = sw[e >> 1][2 * (e & 1) + 1]; \
                const float w[4] = {bf2f(s01 & 0xffff), bf2f(s01 >> 16), bf2f(s23 & 0xffff), bf2f(s23 >> 16)}; \
                float kk[5]; \
_Pragma("unroll") \
                for (int j = 0; j < 5; ++j) kk[j] = bf2f((kr[j][e >> 1] >> (16 * (e & 1))) & 0xffff); \
                float o[2]; \
_Pragma("unroll") \
                for (int j = 0; j < 2; ++j) { \
                    float s = rbf(kk[j] * w[3]); \
                    s = rbf(s + rbf(kk[j + 1] * w[2])); s = rbf(s + rbf(kk[j + 2] * w[1])); s = rbf(s + rbf(kk[j + 3] * w[0])); \
                    o[j] = s * bf2f((vr[j][e >> 1] >> (16 * (e & 1))) & 0xffff); \
                } \
                *(LAS unsigned*)(T + (8 * c5 + e) * 272 + (126 - 2 * tp) * 2) = cvtpk(o[1], o[0]); \
            } \
        } \
        } while (0)
        if (wr == 0) { K1_EPI(); STAMP(5); K1_A1(); } else { K1_A1(); STAMP(5); K1_EPI(); }
#undef K1_EPI
#undef K1_A1
        STAMP(6);
        asm volatile("s_waitcnt vmcnt(16)" : "+v"(xg[0]), "+v"(xg[1]), "+v"(gg[0]), "+v"(gg[1])::"memory");
        float gs = 0.f;
#pragma unroll
        for (int j = 0; j < 2; ++j)
#pragma unroll
            for (int e = 0; e < 8; ++e) gs += rbf(bf2f((xg[j][e >> 1] >> (16 * (e & 1))) & 0xffff) * bf2f((gg[j][e >> 1] >> (16 * (e & 1))) & 0xffff));
#pragma unroll
        for (int o = 32; o >= 1; o >>= 1) gs += __shfl_xor(gs, o);
        const float gt = rbf(1.f / (1.f + __expf(-rbf(gs))));
        STAMP(7);
        WAIT_L(0); BAR();
        STAMP(8);
        const rsrc_t ru = mkrsrc(ur, DM * S * 2), rgo = mkrsrc(gate, S * 8);
#pragma unroll
        for (int r = 0; r < 2; ++r) { const int i = tid + 512 * r, row = i >> 4, ch = i & 15; const bool v = i < 640;
            const u32x4 w = *(LAS u32x4*)(T + (v ? row : 0) * 272 + ch * 16);
            __builtin_amdgcn_raw_buffer_store_b128(w, ru, v ? (unsigned)((d0 + row) * S + (1920 - t0) + ch * 8) * 2u : 0x80000000u, 0, 0); }
        __builtin_amdgcn_raw_buffer_store_b64((u32x2){__float_as_uint(gt), __float_as_uint(rbf(1.f - gt))}, rgo, lane == 0 ? (unsigned)(8 * b + wid) * 8u : 0x80000000u, 0, 0);
#pragma unroll
        for (int j = 0; j < 3; ++j) { const int nt = wid + 8 * j;
            if (nt < 20) {
                const lptr src = lds + (8 * (lane >> 4) + ((lane >> 2) & 3)) * 640 + nt * 32 + (lane & 3) * 8;
                *(bf16x8*)(wt + ((size_t)(b >> 1) * 40 + (b & 1) * 20 + nt) * 1024 + lane * 16) = cat8(trread(src), trread(src + 4 * 640)); } }
    }
    STAMP(3); STAMP_OUT();
#undef SA
#undef SB
#undef STAGE_A
#undef STAGE_B
#undef LDA
#undef LDB
#undef MMA
}

constexpr int K2_LDS = 163840;
constexpr float QSCALE = 0.08838834764831845f * 1.4426950408889634f;
constexpr float SUMTHR = 256.0f;
constexpr int L_K = 0, L_V = 65536, L_PARK = 131072;
#define MPRIO 1
#define SPREAD 1
#define QEXP 0
#define PHOFF 0
constexpr int PFD = 5;
DI float max3f(float a, float b, float c) { float r; asm("v_max3_f32 %0, %1, %2, %3" : "=v"(r) : "v"(a), "v"(b), "v"(c)); return r; }
DI float max2f(float a, float b) { float r; asm("v_max_f32_e32 %0, %1, %2" : "=v"(r) : "v"(a), "v"(b)); return r; }
DI float fadd_s(float a, float b) { float r = a + b; asm("" : "+v"(r)); return r; }
DI void mfma_init(f32x16& d, bf16x8 a, bf16x8 b, const f32x16& c) { asm volatile("v_mfma_f32_32x32x16_bf16 %0, %1, %2, %3" : "=&v"(d) : "v"(a), "v"(b), "v"(c)); }
DI void mfma_zero(f32x16& d, bf16x8 a, bf16x8 b) { asm volatile("v_mfma_f32_32x32x16_bf16 %0, %1, %2, 0" : "=&v"(d) : "v"(a), "v"(b)); }
DI void mfma_acc(f32x16& d, bf16x8 a, bf16x8 b) { asm volatile("v_mfma_f32_32x32x16_bf16 %0, %1, %2, %0" : "+v"(d) : "v"(a), "v"(b)); }
DI void bcast16(f32x16& d, float aone, float v) { asm volatile("s_nop 1\n\tv_mfma_f32_32x32x2_f32 %0, %1, %2, 0" : "+v"(d) : "v"(aone), "v"(v)); }
DI void warm_line(unsigned ldsdummy, rsrc_t r, unsigned voff) { asm volatile("s_mov_b32 m0, %0\n\ts_nop 0\n\tbuffer_load_dword %1, %2, 0 offen lds" ::"s"(ldsdummy), "v"(voff), "s"(r) : "memory"); }

constexpr int A2_HC = 4160, A2_U = 4 * A2_HC, A2_UH = 79 * 32, A2_CH = A2_U + 2 * A2_UH;
typedef unsigned u32x6 __attribute__((ext_vector_type(6)));

__global__ __launch_bounds__(512, 2) void k2_attn(const u16* __restrict__ x, const u16* __restrict__ wq, const char* __restrict__ Kg, const char* __restrict__ Vg, char* __restrict__ ctxf,
                                                  const u16* __restrict__ ur, const u16* __restrict__ ssmk, const u16* __restrict__ ssmD, u16* __restrict__ zT DBGARG) {
    STAMP_DECL; STAMP(0);
    extern __shared__ __attribute__((aligned(16))) char smem[];
    const lptr lds = (lptr)smem;
    const unsigned ldsb = (unsigned)(uintptr_t)smem;
    const int tid = threadIdx.x, lane = tid & 63, r32 = lane & 31, hi = lane >> 5;
    const int wid = rfl(tid >> 6), grp = wid >> 2, wq4 = wid & 3;
    const int b = blockIdx.x, head = 4 * (b & 7) + (b >> 6), xi = (b >> 3) & 7, j32 = b >> 3;
    const int blkB = 15 - xi, blkA = xi, x2 = 2 * xi;
    const rsrc_t rK = mkrsrc(Kg + (size_t)head * 524288, 524288), rV = mkrsrc(Vg + (size_t)head * 524288, 524288);
    const unsigned vlin = (unsigned)lane * 16u;
    bf16x8 q[8];
    {
        const rsrc_t rX = mkrsrc(x, S * DM * 2), rW = mkrsrc(wq, DM * 4096 * 2);
        unsigned vx[2];
#pragma unroll
        for (int j = 0; j < 2; ++j) vx[j] = (unsigned)(lane >> 3) * 1280u + (unsigned)(((lane & 7) ^ (4 * j + (lane >> 4))) * 16);
        const unsigned vw = (unsigned)((lane >> 2) & 7) * 8192u + (unsigned)(lane >> 5) * 64u + (unsigned)(lane & 3) * 16u;
        const unsigned sx0 = (unsigned)((grp ? blkA : blkB) * 128 + wq4 * 32) * 1280u, sw0 = (unsigned)wid * 8u * 8192u + (unsigned)head * 256u;
#define QPIECE(p, t, sb_) do { if ((p) < 4) dma16(rX, vx[(p) & 1], sx0 + (p) * 8u * 1280u + (unsigned)(t) * 128u, (sb_) + wid * 4096u + (p) * 1024u); \
                               else dma16(rW, vw, sw0 + (unsigned)(t) * 64u * 8192u + ((p) & 1) * 128u, (sb_) + 32768u + (wid * 2 + ((p) & 1)) * 1024u); } while (0)
        const unsigned dummy = ldsb + 147456u;
        {
            const int nch = b < 128 ? 3 : 2, ch0 = b < 128 ? 3 * b : 384 + 2 * (b - 128);
            const rsrc_t rH = mkrsrc(ssmk + (size_t)ch0 * S, (unsigned)nch * S * 2), rU = mkrsrc(ur + (size_t)ch0 * S, (unsigned)nch * S * 2);
            u32x4 hlo[2], hhi[2];
#pragma unroll
            for (int rd = 0; rd < 2; ++rd) {
                const int task = tid + 512 * rd, c = task / 260, ck = task - 260 * c, n0 = 8 * ck - 32;
                const bool on = c < nch;
                ld16a(hlo[rd], rH, (on && n0 >= 0) ? (unsigned)(c * S + n0) * 2u : 0x80000000u);
                ld16a(hhi[rd], rH, (on && n0 + 8 >= 0 && n0 + 8 < S) ? (unsigned)(c * S + n0 + 8) * 2u : 0x80000000u);
            }
            if (wid < nch) {
#pragma unroll
                for (int p = 0; p < 4; ++p) { const int J = 32 * (p & 1) + (lane >> 1), g = 2 * (p >> 1) + (lane & 1);
                    dma16(rU, (unsigned)(4 * (63 - J) + g) * 16u, (unsigned)wid * (S * 2u), ldsb + wid * A2_CH + A2_U + (p >> 1) * A2_UH + (15 + 32 * (p & 1)) * 32); }
            }
#pragma unroll
            for (int p = 0; p < 6; ++p) QPIECE(p, 0, ldsb + 2 * 49152u);
            {
                const int i = tid - 192, l = 48 * j32 + i;
                const unsigned ox = (unsigned)(64 * j32 + (tid & 63)) * 1280u + (unsigned)(1 + (tid >> 6)) * 128u;
                const unsigned ow = (unsigned)(64 + (l >> 3)) * 8192u + (unsigned)(b & 7) * 1024u + (unsigned)(l & 7) * 128u;
                if (wid < 3) warm_line(dummy, rX, ox); else warm_line(dummy, rW, tid < 240 ? ow : 0x80000000u);
            }
            if (wid < nch && lane < 60) *(LAS u32x4*)(lds + wid * A2_CH + A2_U + (lane / 30) * A2_UH + (lane % 30) * 16) = (u32x4){0, 0, 0, 0};
            asm volatile("s_waitcnt vmcnt(7)" : "+v"(hlo[0]), "+v"(hhi[0]), "+v"(hlo[1]), "+v"(hhi[1])::"memory");
#pragma unroll
            for (int rd = 0; rd < 2; ++rd) {
                const int task = tid + 512 * rd, c = task / 260, ck = task - 260 * c;
                if (c < nch) {
                    const unsigned r[6] = {hlo[rd].x, hlo[rd].y, hlo[rd].z, hlo[rd].w, hhi[rd].x, hhi[rd].y};
                    unsigned sh[5];
#pragma unroll
                    for (int i = 0; i < 5; ++i) sh[i] = __builtin_amdgcn_alignbyte(r[i + 1], r[i], 2);
                    const lptr dst = lds + c * A2_CH + ck * 16;
                    *(LAS u32x4*)(dst) = (u32x4){sh[0], sh[1], sh[2], sh[3]};
                    *(LAS u32x4*)(dst + A2_HC) = (u32x4){r[1], r[2], r[3], r[4]};
                    *(LAS u32x4*)(dst + 2 * A2_HC) = (u32x4){sh[1], sh[2], sh[3], sh[4]};
                    *(LAS u32x4*)(dst + 3 * A2_HC) = (u32x4){r[2], r[3], r[4], r[5]};
                }
            }
            WAIT_L(0); BAR();
            STAMP(4);
            if (wid < nch) {
                const lptr cb = lds + wid * A2_CH;
                const int i = lane & 15, g = lane >> 4;
                const lptr ab = cb + (i & 3) * A2_HC + ((i >> 2) + g) * 16;
                const lptr bb = cb + A2_U + (g >> 1) * A2_UH + (i + 15) * 32 + (g & 1) * 16;
                f32x4 acc[4][2] = {};
                u32x4 alo[2][4], bq[2][4]; u32x2 aup[2][4];
#define A2LOAD(X, f) do { _Pragma("unroll") for (int c = 0; c < 4; ++c) bq[X][c] = *(LAS u32x4*)(bb + (16 * c - (f)) * 32); \
                    _Pragma("unroll") for (int e = 0; e < 4; ++e) { alo[X][e] = *(LAS u32x4*)(ab + (16 * e + (f)) * 64); aup[X][e] = *(LAS u32x2*)(ab + (16 * e + (f)) * 64 + 16); } SBAR(); } while (0)
#define A2MMA(X) do { _Pragma("unroll") for (int e = 0; e < 4; ++e) { \
                        const u32x6 w6 = {alo[X][e].x, alo[X][e].y, alo[X][e].z, alo[X][e].w, aup[X][e].x, aup[X][e].y}; \
                        const bf16x8 a0 = __builtin_bit_cast(bf16x8, __builtin_shufflevector(w6, w6, 0, 1, 2, 3)), a1 = __builtin_bit_cast(bf16x8, __builtin_shufflevector(w6, w6, 2, 3, 4, 5)); \
                        _Pragma("unroll") for (int c = e; c < 4; ++c) { const bf16x8 bv = __builtin_bit_cast(bf16x8, bq[X][c - e]); acc[c][0] = MFMA16(a0, bv, acc[c][0]); acc[c][1] = MFMA16(a1, bv, acc[c][1]); } } SBAR(); } while (0)
                A2LOAD(0, 0);
                for (int f = 0; f < 16; f += 2) {
                    A2LOAD(1, f + 1);
                    A2MMA(0);
                    A2LOAD(0, (f + 2) & 15);
                    A2MMA(1);
                }
#undef A2LOAD
#undef A2MMA
                const int d = ch0 + wid;
                const float D = bf2f(ssmD[d]);
                u32x4 zw[4];
#pragma unroll
                for (int c = 0; c < 4; ++c) {
                    const u32x4 uu = *(LAS u32x4*)(cb + A2_U + ((3 - g) >> 1) * A2_UH + (16 * c + i + 15) * 32 + ((3 - g) & 1) * 16);
                    float z[8];
#pragma unroll
                    for (int e = 0; e < 8; ++e) { const int ep = 7 - e; const float u = bf2f((uu[ep >> 1] >> (16 * (ep & 1))) & 0xffff);
                        z[e] = rbf(acc[c][e >> 2][e & 3]) + rbf(u * D); }
                    zw[c] = (u32x4){cvtpk(z[0], z[1]), cvtpk(z[2], z[3]), cvtpk(z[4], z[5]), cvtpk(z[6], z[7])};
                }
                asm volatile("s_waitcnt vmcnt(1)" : "+v"(zw[0]), "+v"(zw[1]), "+v"(zw[2]), "+v"(zw[3])::"memory");
                const rsrc_t rZ = mkrsrc(zT + (size_t)d * S, S * 2);
#pragma unroll
                for (int c = 0; c < 4; ++c) __builtin_amdgcn_raw_buffer_store_b128(zw[c], rZ, (unsigned)(32 * (16 * c + i) + 8 * g) * 2u, 0, 0);
            } else WAIT_V(1);
            WAIT_L(0);
        }
        STAMP(5);
        const int row = wid * 32 + r32;
        const int xb = row * 128 + ((hi ^ ((row >> 1) & 7)) << 4);
        const int wb = 32768 + 8 * (lane & 3) + 64 * ((lane >> 2) & 3) + 32 * ((lane >> 4) & 1) + 2048 * hi;
        f32x16 qa[4] = {};
        asm volatile("" : "+v"(qa[0]), "+v"(qa[1]), "+v"(qa[2]), "+v"(qa[3]));
        const int wl = wid * 10 + lane;
        unsigned wo = wid < 7 ? ((lane < 10 && wl < 64) ? (unsigned)(64 * j32 + wl) * 1280u + 512u : 0x80000000u)
                              : (lane < 16 ? (unsigned)(256 + 2 * j32 + (lane >> 3)) * 8192u + (unsigned)(b & 7) * 1024u + (unsigned)(lane & 7) * 128u : 0x80000000u);
        const unsigned wostep = wid < 7 ? 128u : 64u * 8192u;
        const rsrc_t rWarm = wid < 7 ? rX : rW;
        unsigned vx4[4];
#pragma unroll
        for (int p = 0; p < 4; ++p) vx4[p] = vx[p & 1] + p * (8u * 1280u - 1024u);
        bf16x8 wf[16 + PFD], xf[6];
#define QFRAG(j, so_, jj) do { if (((jj) & 3) == 0) xf[(j) >> 2] = ldsfrag(lds + (so_) + (xb ^ (((jj) >> 2) << 5))); \
                      wf[j] = cat8(trread(lds + (so_) + wb + 4096 * ((jj) >> 2) + 512 * ((jj) & 3)), trread(lds + (so_) + wb + 4096 * ((jj) >> 2) + 512 * ((jj) & 3) + 256)); } while (0)
#define QTILE(KIND, PRE, t, st) do { \
            if (KIND == 1 || KIND == 2) WAIT_V(1); \
            BAR(); \
            if (PHOFF) { PHM0(t); PH(0); } \
            const int so = (st) * 49152, son = ((st) == 2 ? 0 : (st) + 1) * 49152; \
            const unsigned sb2 = ldsb + ((st) == 0 ? 2 : (st) - 1) * 49152u;     \
            const unsigned sxt = sx0 + (unsigned)((t) + 2) * 128u, swt = sw0 + (unsigned)((t) + 2) * 64u * 8192u; \
            if (!(PRE)) { _Pragma("unroll") for (int j = 0; j < PFD; ++j) QFRAG(j, so, j); } \
            else { _Pragma("unroll") for (int j = 0; j < PFD; ++j) wf[j] = wf[16 + j]; xf[0] = xf[4]; xf[1] = xf[5]; } \
            SBAR(); \
            _Pragma("unroll") for (int j = 0; j < 16; ++j) { \
                mfma_acc(qa[j & 3], wf[j], xf[j >> 2]); \
                if (j + PFD < 16) QFRAG(j + PFD, so, j + PFD); else if (KIND != 3) QFRAG(j + PFD, son, j + PFD - 16); \
                if (KIND == 0) { if (j < 6) QPIECE(j, 1, ldsb); else if (j < 12) QPIECE(j - 6, 2, ldsb + 49152u); } \
                if (KIND == 1) { \
                    if (j == 0) { setm0(sb2 + wid * 4096u); dma16o<0>(rX, vx4[0], sxt); } \
                    if (j == 1) dma16o<1024>(rX, vx4[1], sxt); if (j == 2) dma16o<2048>(rX, vx4[2], sxt); if (j == 3) dma16o<3072>(rX, vx4[3], sxt); \
                    if (j == 4) { setm0(sb2 + 32768u + wid * 2048u); dma16o<0>(rW, vw, swt); } \
                    if (j == 5) dma16o<1024>(rW, vw, swt - 896u); } \
                if (KIND == 2) { if (j == 0) dma16x2(rK, vlin, wid * 2048u, ldsb + L_K + wid * 2048u); if (j == 2) dma16x2(rK, vlin, 16384u + wid * 2048u, ldsb + L_K + 16384u + wid * 2048u); } \
                if (KIND == 3) { if (j == 0) dma16x2(rV, vlin, wid * 2048u, ldsb + L_V + wid * 2048u); } \
                if (KIND <= 1 && j == 12) { warm_line(dummy, rWarm, wo); wo = (t) < 5 ? wo + wostep : 0x80000000u; } \
                SBAR(); \
            } } while (0)
        PHINIT();
        QTILE(0, false, 0, 2);
        QTILE(1, false, 1, 0);
        { int st = 1;
          for (int t = 2; t < 8; ++t) { QTILE(1, true, t, st); st = st == 2 ? 0 : st + 1; } }
        QTILE(2, true, 8, 1);
        QTILE(3, true, 9, 2);
#undef QTILE
#undef QFRAG
#undef QPIECE
        asm volatile("s_nop 7\n\ts_nop 7" : "+v"(qa[0]), "+v"(qa[1]), "+v"(qa[2]), "+v"(qa[3]));
#pragma unroll
        for (int s = 0; s < 8; ++s) {
            u32x4 w;
#pragma unroll
            for (int e = 0; e < 4; ++e) w[e] = cvtpk(qa[s >> 1][8 * (s & 1) + 2 * e] * QSCALE, qa[s >> 1][8 * (s & 1) + 2 * e + 1] * QSCALE);
            q[s] = __builtin_bit_cast(bf16x8, w);
        }
        WAIT_L(0); BAR();
        if (!grp) {
            const lptr dst = lds + L_PARK + wq4 * 8192 + lane * 16;
#pragma unroll
            for (int s = 0; s < 8; ++s) *(LAS bf16x8*)(dst + s * 1024) = q[s];
        }
        WAIT_L(0);
        WAIT_V(0);
        BAR();
    }
    STAMP(1);
    const int kl = L_K + hi * 1024 + r32 * 16, vl = L_V + 8 * (lane & 3) + 64 * ((lane >> 2) & 3) + 32 * ((lane >> 4) & 1) + 256 * hi;
    const unsigned kdst = ldsb + L_K + wq4 * 4096, vdst = ldsb + L_V + wq4 * 4096;
#define TILE(s_) ((s_) <= x2 + 1 ? (s_) : 2 * (s_) - x2 - 2 + grp)
    f32x16 o[4] = {};
    f32x16 s0, s1, negm = {};
    asm volatile("" : "+v"(negm), "+v"(o[0]), "+v"(o[1]), "+v"(o[2]), "+v"(o[3]));
    float mref = 0.f, lsum = 0.f, lsumA = 0.f;
    bf16x8 fr_[32], pp[4];
    int kb = kl, vb = vl;
    int qrow = (grp ? blkA : blkB) * 128 + wq4 * 32;
    const unsigned kmask = rfl(grp ? (0x7fffu >> x2) << x2 : 0x7fffu), vmask = rfl(grp ? 0xffffu : (0xffffu >> (x2 + 1)) << (x2 + 1)), spmask = rfl(1u | (15u << x2) | 0x10000u);
    unsigned ks1 = 16384u + wq4 * 4096u, ks2 = rfl((unsigned)TILE(2) * 16384u + wq4 * 4096u), ko0 = 0u, ko1 = 16384u, ko2 = rfl(((unsigned)TILE(2) & 3u) * 16384u);
#define ROLL(s_) do { const unsigned t3 = (unsigned)TILE((s_) + 2) << 14; ko0 = ko1; ko1 = ko2; ks1 = ks2; ks2 = t3 + wq4 * 4096u; ko2 = t3 & 0xc000u; \
        asm volatile("" : "+s"(ks1), "+s"(ks2), "+s"(ko0), "+s"(ko1), "+s"(ko2)); } while (0)
#define FRAG(i) ((i) < 16 ? ldsfrag(lds + kb + ((i) >> 1) * 2048 + ((i) & 1) * 512) \
                          : cat8(trread(lds + vb + (((i) - 16) >> 2) * 4096 + ((i) & 3) * 512), trread(lds + vb + (((i) - 16) >> 2) * 4096 + ((i) & 3) * 512 + 2048)))
#define QK(LAST, NEXT) do { _Pragma("unroll") for (int i = 0; i < 16; ++i) { \
            if (i < 2) mfma_init(i ? s1 : s0, fr_[i], q[0], negm); else mfma_acc((i & 1) ? s1 : s0, fr_[i], q[i >> 1]); \
            if (i + PFD < (LAST)) fr_[i + PFD] = FRAG(i + PFD); \
            if ((NEXT) >= 0 && i == 4) ROLL(NEXT); \
            SBAR(); } } while (0)
#define PV() do { _Pragma("unroll") for (int i = 0; i < 16; ++i) { \
            mfma_acc(o[i & 3], fr_[16 + i], pp[i >> 2]); \
            if (i + PFD < 16) fr_[16 + i + PFD] = FRAG(16 + i + PFD); \
            SBAR(); } } while (0)
#define EXPSUM() do { float la = 0.f, lb = 0.f; \
            _Pragma("unroll") for (int ks = 0; ks < 4; ++ks) { u32x4 w; \
                _Pragma("unroll") for (int e = 0; e < 4; ++e) { const int r = 8 * (ks & 1) + 2 * e; \
                    const float ea = __builtin_amdgcn_exp2f((ks >> 1) ? s1[r] : s0[r]), eb = __builtin_amdgcn_exp2f((ks >> 1) ? s1[r + 1] : s0[r + 1]); \
                    la = (ks | e) ? fadd_s(la, ea) : ea; lb = (ks | e) ? fadd_s(lb, eb) : eb; w[e] = cvtpk(ea, eb); } \
                pp[ks] = __builtin_bit_cast(bf16x8, w); } \
            tsum = la + lb; } while (0)
#define ROWMAX(rm) do { float ma = max3f(s0[0], s0[1], s1[0]), mb = max3f(s0[2], s0[3], s1[1]); ma = max3f(ma, s1[2], s1[3]); \
            _Pragma("unroll") for (int r = 4; r < 16; r += 4) { ma = max3f(ma, s0[r], s0[r + 1]); mb = max3f(mb, s0[r + 2], s0[r + 3]); ma = max3f(ma, s1[r], s1[r + 1]); mb = max3f(mb, s1[r + 2], s1[r + 3]); } \
            rm = max2f(ma, mb); \
            auto rr_ = __builtin_amdgcn_permlane32_swap(__float_as_uint(rm), __float_as_uint(rm), false, false); rm = max2f(__uint_as_float(rr_[0]), __uint_as_float(rr_[1])); } while (0)
#define STORE_A() do { const lptr tb = lds + L_PARK + wq4 * 8192; \
            char* cb = ctxf + ((size_t)(blkA * 8 + wq4 * 2) * 128 + head * 4) * 1024 + lane * 16; \
            _Pragma("unroll") for (int i = 0; i < 8; ++i) { const int rr = 16 * (i >> 2) + (lane & 15), ch = 4 * (i & 3) + (lane >> 4); \
                *(u32x4*)(cb + (size_t)(i >> 2) * 131072 + (i & 3) * 1024) = *(LAS u32x4*)(tb + rr * 256 + ((ch ^ (rr & 15)) << 4)); } } while (0)
#define VPHASE(LASTT) do { \
            if ((kmask >> s) & 1u) dma16x4(rK, vlin, ks2, kdst + ko2); \
            if ((vmask >> s) & 1u) dma16x4(rV, vlin, ks1, vdst + ko1); \
            const bool sp = (spmask >> s) & 1u; \
            if (__builtin_expect(sp, 0)) { \
                asm volatile("s_nop 7\n\ts_nop 7" : "+v"(o[0]), "+v"(o[1]), "+v"(o[2]), "+v"(o[3]), "+v"(s0), "+v"(s1)); \
                bool first = s == 0; \
                if (grp && s == x2 + 2) {     \
                    const float rl = __builtin_amdgcn_rcpf(lsumA); \
                    const lptr tb = lds + L_PARK + wq4 * 8192; \
                    _Pragma("unroll") for (int c = 0; c < 8; ++c) { const int d = c >> 1, r = (c & 1) * 8; u32x4 w; \
                        w.x = cvtpk(o[d][r] * rl, o[d][r + 1] * rl); w.y = cvtpk(o[d][r + 2] * rl, o[d][r + 3] * rl); w.z = cvtpk(o[d][r + 4] * rl, o[d][r + 5] * rl); w.w = cvtpk(o[d][r + 6] * rl, o[d][r + 7] * rl); \
                        *(LAS u32x4*)(tb + r32 * 256 + (((2 * c + hi) ^ (r32 & 15)) << 4)) = w; } \
                    _Pragma("unroll") for (int d = 0; d < 4; ++d) o[d] = (f32x16){}; \
                    first = true; \
                } \
                if (grp && s == x2 + 3) STORE_A(); \
                const int rel = qrow - 64 * TILE(s);     \
                if (rel <= 32) { const int th = r32 - 4 * hi; \
                    if (rel == 32) { _Pragma("unroll") for (int r = 0; r < 16; ++r) if ((r & 3) + 8 * (r >> 2) > th) s1[r] = -INFINITY; } \
                    else { if (rel == 0) { _Pragma("unroll") for (int r = 0; r < 16; ++r) if ((r & 3) + 8 * (r >> 2) > th) s0[r] = -INFINITY; } \
                           else { _Pragma("unroll") for (int r = 0; r < 16; ++r) s0[r] = -INFINITY; } \
                           _Pragma("unroll") for (int r = 0; r < 16; ++r) s1[r] = -INFINITY; } } \
                if (first) {     \
                    float rm; ROWMAX(rm); \
                    mref = __builtin_fmaxf(rm, -1e30f); \
                    _Pragma("unroll") for (int r = 0; r < 16; ++r) { s0[r] -= mref; s1[r] -= mref; } \
                    bcast16(negm, hi ? 0.f : 1.f, -mref); \
                } \
            } \
            float tsum; \
            EXPSUM(); \
            if (__builtin_expect(__any(!(tsum <= SUMTHR)), 0)) {     \
                float rm; ROWMAX(rm); \
                const float dl = __builtin_fmaxf(rm, 0.f), f = __builtin_amdgcn_exp2f(-dl); \
                mref += dl; lsum *= f; \
                _Pragma("unroll") for (int r = 0; r < 16; ++r) { s0[r] -= dl; s1[r] -= dl; } \
                bcast16(negm, hi ? 0.f : 1.f, -mref); \
                _Pragma("unroll") for (int d = 0; d < 4; ++d) _Pragma("unroll") for (int r = 0; r < 16; ++r) o[d][r] *= f; \
                EXPSUM(); \
            } \
            lsum += tsum; \
            if (__builtin_expect(sp, 0)) { if (grp && s == x2 + 1) {     \
                auto rr = __builtin_amdgcn_permlane32_swap(__float_as_uint(lsum), __float_as_uint(lsum), false, false); lsumA = __uint_as_float(rr[0]) + __uint_as_float(rr[1]); \
                _Pragma("unroll") for (int u = 0; u < 8; ++u) q[u] = ldsfrag(lds + L_PARK + wq4 * 8192 + lane * 16 + u * 1024); \
                bcast16(negm, 0.f, 0.f); mref = 0.f; lsum = 0.f; qrow = blkB * 128 + wq4 * 32; \
                WAIT_L(0); } } \
            asm volatile("" : "+v"(pp[0]), "+v"(pp[1]), "+v"(pp[2]), "+v"(pp[3]), "+v"(lsum)); \
            SBAR(); \
            vb = vl + ko0; kb = kl + ko1; \
            _Pragma("unroll") for (int u = 0; u < PFD; ++u) fr_[((LASTT) ? 16 : 0) + u] = FRAG(((LASTT) ? 16 : 0) + u); \
            } while (0)
    if (grp) BAR();
    {
#pragma unroll
        for (int u = 0; u < PFD; ++u) fr_[u] = FRAG(u);
        SBAR();
        __builtin_amdgcn_s_setprio(MPRIO);
        QK(16, -1);
        __builtin_amdgcn_s_setprio(0);
    }
    int s = 0;
    PHINIT();
    for (; s < 16; ++s) {
        WAIT_V(0); BAR();
        if (!PHOFF) { PHM0(4 * s + 0); PH(0); }
        VPHASE(false);
        if (!PHOFF) { PHM0(4 * s + 1); PH(1); }
        BAR();
        if (!PHOFF) { PHM0(4 * s + 2); PH(2); }
        __builtin_amdgcn_s_setprio(MPRIO);
        QK(32, s + 1);
        PV();
        __builtin_amdgcn_s_setprio(0);
        if (!PHOFF) { PHM0(4 * s + 3); PH(3); }
    }
    WAIT_V(0); BAR();
    VPHASE(true);
    BAR();
    __builtin_amdgcn_s_setprio(MPRIO);
    PV();
    __builtin_amdgcn_s_setprio(0);
    asm volatile("s_nop 7\n\ts_nop 7" : "+v"(o[0]), "+v"(o[1]), "+v"(o[2]), "+v"(o[3]));
    if (grp && x2 + 3 > 16) { WAIT_L(0); STORE_A(); }
#undef FRAG
#undef VPHASE
#undef EXPSUM
#undef ROWMAX
#undef STORE_A
#undef QK
#undef PV
#undef ROLL
#undef TILE
    STAMP(2);
    { auto rr = __builtin_amdgcn_permlane32_swap(__float_as_uint(lsum), __float_as_uint(lsum), false, false); lsum = __uint_as_float(rr[0]) + __uint_as_float(rr[1]); }
    if (!grp) BAR();
    BAR();
    LAS f32x2* ml = (LAS f32x2*)(lds + 65536);
    if (hi == 0) ml[wid * 32 + r32] = (f32x2){mref, lsum};
#define MERGE(KEEP, SEND) do { \
    { const lptr dst = lds + wid * 8192 + lane * 16; \
      _Pragma("unroll") for (int e = 0; e < 8; ++e) { const int d = SEND + (e >> 2), r = (e & 3) * 4; \
          *(LAS f32x4*)(dst + e * 1024) = (f32x4){o[d][r], o[d][r + 1], o[d][r + 2], o[d][r + 3]}; } } \
    WAIT_L(0); BAR(); \
    const f32x2 oth = ml[(wid ^ 4) * 32 + r32]; \
    const float mm = __builtin_fmaxf(mref, oth.x), w0 = __builtin_amdgcn_exp2f(mref - mm), w1 = __builtin_amdgcn_exp2f(oth.x - mm); \
    const float rl = __builtin_amdgcn_rcpf(lsum * w0 + oth.y * w1), sc0 = w0 * rl, sc1 = w1 * rl; \
    { const lptr src = lds + (wid ^ 4) * 8192 + lane * 16; \
      u32x4 w[4]; \
      _Pragma("unroll") for (int c = 0; c < 4; ++c) {     \
          const int d = KEEP + (c >> 1), r = (c & 1) * 8; \
          const f32x4 a0 = *(LAS f32x4*)(src + (2 * c) * 1024), a1 = *(LAS f32x4*)(src + (2 * c + 1) * 1024); \
          w[c].x = cvtpk(o[d][r] * sc0 + a0[0] * sc1, o[d][r + 1] * sc0 + a0[1] * sc1); w[c].y = cvtpk(o[d][r + 2] * sc0 + a0[2] * sc1, o[d][r + 3] * sc0 + a0[3] * sc1); \
          w[c].z = cvtpk(o[d][r + 4] * sc0 + a1[0] * sc1, o[d][r + 5] * sc0 + a1[1] * sc1); w[c].w = cvtpk(o[d][r + 6] * sc0 + a1[2] * sc1, o[d][r + 7] * sc0 + a1[3] * sc1); } \
        \
      WAIT_L(0); \
      const lptr tb = lds + (wid ^ 4) * 8192; \
      _Pragma("unroll") for (int c = 0; c < 4; ++c) *(LAS u32x4*)(tb + r32 * 128 + (((2 * c + hi) ^ (r32 & 7)) << 4)) = w[c]; \
      WAIT_L(0); \
      char* cb = ctxf + ((size_t)(blkB * 8 + wq4 * 2) * 128 + head * 4 + KEEP) * 1024 + lane * 16; \
      _Pragma("unroll") for (int i = 0; i < 4; ++i) { const int rr = 16 * (i >> 1) + (lane & 15), ch = 4 * (i & 1) + (lane >> 4); \
          *(u32x4*)(cb + (size_t)(i >> 1) * 131072 + (i & 1) * 1024) = *(LAS u32x4*)(tb + rr * 128 + ((ch ^ (rr & 7)) << 4)); } } } while (0)
    if (grp) MERGE(2, 0); else MERGE(0, 2);
#undef MERGE
    STAMP(3);
    STAMP_OUT();
}

constexpr int K3_T2 = 140 * 1024, K3_LDS = K3_T2 + 64 * 176;
__global__ __launch_bounds__(512, 2) void k3_oproj(const char* __restrict__ ctxf, const char* __restrict__ wt, const u16* __restrict__ x, const u16* __restrict__ zT,
                                                   const float* __restrict__ gate, u16* __restrict__ out DBGARG) {
    STAMP_DECL; STAMP(0);
    extern __shared__ __attribute__((aligned(16))) char smem[];
    const lptr lds = (lptr)smem;
    const int tid = threadIdx.x, lane = tid & 63, fr = lane & 15, fq = lane >> 4;
    const int wid = rfl(tid >> 6);
    const int b = blockIdx.x, xcd = b & 7, j32 = b >> 3, pm = 8 * (xcd >> 1) + (j32 & 7), pn = 4 * (xcd & 1) + (j32 >> 3);
    const rsrc_t rA = mkrsrc(ctxf, S * 4096 * 2), rB = mkrsrc(wt, 4096 * DM * 2);
    const unsigned vlin = (unsigned)lane * 16u, sa = (unsigned)((4 * pm) * 128 + 16 * wid) * 1024u, sb = (unsigned)((16 * wid) * 40 + 5 * pn) * 1024u;
    f32x4 acc[4][5] = {};
    bf16x8 af[3][4], bfr[3][5];
#define LOADF(X, s_, i) do { if ((i) < 4) af[X][(i) & 3] = __builtin_bit_cast(bf16x8, __builtin_amdgcn_raw_buffer_load_b128(rA, vlin, sa + ((i) & 3) * 131072u + (s_) * 1024u, 0)); \
                             else bfr[X][((i) - 4) % 5] = __builtin_bit_cast(bf16x8, __builtin_amdgcn_raw_buffer_load_b128(rB, vlin + (((i) - 4) % 5) * 1024u, sb + (s_) * 40960u, 0)); } while (0)
#pragma unroll
    for (int s = 0; s < 2; ++s)
#pragma unroll
        for (int i = 0; i < 9; ++i) LOADF(s, s, (i == 0 ? 4 : (i <= 4 ? i - 1 : i)));
    const int t0 = pm * 64, c0 = pn * 80;
    u32x4 zr[2]; u32x2 xq[3]; f32x2 gv[3];
#pragma unroll
    for (int r = 0; r < 2; ++r) { const int i = tid + 512 * r; zr[r] = i < 640 ? *(const u32x4*)(zT + (size_t)(c0 + (i >> 3)) * S + t0 + (i & 7) * 8) : (u32x4){0, 0, 0, 0}; }
#pragma unroll
    for (int u = 0; u < 3; ++u) { const int e = (wid + 8 * u) < 20 ? wid + 8 * u : wid;
        xq[u] = *(const u32x2*)(x + (size_t)(t0 + (e & 3) * 16 + fr) * DM + c0 + (e >> 2) * 16 + fq * 4); gv[u] = *(const f32x2*)(gate + (size_t)(t0 + (e & 3) * 16 + fr) * 2); }
    SBAR();
#pragma unroll
    for (int s = 0; s < 16; ++s) {
#pragma unroll
        for (int j = 0; j < 20; ++j) {
            acc[j & 3][j >> 2] = MFMA16(bfr[s % 3][j >> 2], af[s % 3][j & 3], acc[j & 3][j >> 2]);
            if (s + 2 < 16 && (j & 1) == 0 && j < 18) { const int i = j >> 1; LOADF((s + 2) % 3, s + 2, (i == 0 ? 4 : (i <= 4 ? i - 1 : i))); }
            SBAR();
        }
        if (s == 1) STAMP(1);
    }
#undef LOADF
    STAMP(2);
#pragma unroll
    for (int r = 0; r < 2; ++r) { const int i = tid + 512 * r;
        if (i < 640) {
#pragma unroll
            for (int e = 0; e < 8; ++e) *(LAS u16*)(lds + K3_T2 + ((i & 7) * 8 + e) * 176 + (i >> 3) * 2) = (u16)((zr[r][e >> 1] >> (16 * (e & 1))) & 0xffff); } }
#pragma unroll
    for (int e = 0; e < 20; ++e) if ((e & 7) != wid) *(LAS f32x4*)(lds + (e * 7 + (wid < (e & 7) ? wid : wid - 1)) * 1024 + lane * 16) = acc[e & 3][e >> 2];
    f32x4 own[3];
#define OWN(W) case W: own[0] = acc[(W) & 3][(W) >> 2]; own[1] = acc[((W) + 8) & 3][((W) + 8) >> 2]; own[2] = acc[(((W) & 3) + 16) & 3][(((W) & 3) + 16) >> 2]; break;
    switch (wid) { OWN(0) OWN(1) OWN(2) OWN(3) OWN(4) OWN(5) OWN(6) default: OWN(7) }
#undef OWN
    WAIT_L(0); BAR();
    STAMP(3);
#pragma unroll
    for (int u = 0; u < 3; ++u) {
        const int e = wid + 8 * u;
        if (e < 20) {
            const lptr src = lds + e * 7 * 1024 + lane * 16;
            f32x4 p[8];
            f32x4 sum = {0.f, 0.f, 0.f, 0.f};
#pragma unroll
            for (int r = 0; r < 7; ++r) p[r] = *(LAS f32x4*)(src + r * 1024);
#pragma unroll
            for (int w = 0; w < 8; ++w) sum += w == wid ? own[u] : (w < wid ? p[w < 7 ? w : 6] : p[w > 0 ? w - 1 : 0]);
            const int row = (e & 3) * 16 + fr, col = (e >> 2) * 16 + fq * 4;
            const u32x2 zz = *(LAS u32x2*)(lds + K3_T2 + row * 176 + col * 2);
            float o[4];
#pragma unroll
            for (int j = 0; j < 4; ++j) {
                const float a = rbf(bf2f((xq[u][j >> 1] >> (16 * (j & 1))) & 0xffff) * bf2f((zz[j >> 1] >> (16 * (j & 1))) & 0xffff));
                o[j] = rbf(gv[u].x * a) + rbf(gv[u].y * rbf(sum[j]));
            }
            *(u32x2*)(out + (size_t)(t0 + row) * DM + c0 + col) = (u32x2){cvtpk(o[0], o[1]), cvtpk(o[2], o[3])};
        }
    }
    STAMP(4); STAMP_OUT();
}

extern "C" void kernel_launch(void* const* d_in, const int* in_sizes, int n_in, void* d_out, int out_size, void* d_ws, size_t ws_size, hipStream_t stream) {
    (void)in_sizes; (void)n_in; (void)out_size; (void)ws_size;
    const u16* Aq = (const u16*)d_in[0]; const u16* Ak = (const u16*)d_in[1]; const u16* Av = (const u16*)d_in[2];
    const u16* shk = (const u16*)d_in[3]; const u16* ssmk = (const u16*)d_in[4]; const u16* ssmD = (const u16*)d_in[5];
    const u16* ckv = (const u16*)d_in[7]; const u16* wq = (const u16*)d_in[8]; const u16* wuk = (const u16*)d_in[9];
    const u16* wuv = (const u16*)d_in[10]; const u16* wo = (const u16*)d_in[11]; const u16* gw = (const u16*)d_in[12];
    char* ws = (char*)d_ws;
    (void)hipFuncSetAttribute((const void*)k1_kvproj, hipFuncAttributeMaxDynamicSharedMemorySize, K1_LDS);
    (void)hipFuncSetAttribute((const void*)k2_attn, hipFuncAttributeMaxDynamicSharedMemorySize, K2_LDS);
    (void)hipFuncSetAttribute((const void*)k3_oproj, hipFuncAttributeMaxDynamicSharedMemorySize, K3_LDS);
    k1_kvproj<<<256, 512, K1_LDS, stream>>>(ckv, wuk, wuv, ws + WS_K, ws + WS_V, Ak, Av, shk, (u16*)(ws + WS_UR), Aq, gw, (float*)(ws + WS_GATE), wo, ws + WS_WT DBGPASS(0));
    k2_attn<<<256, 512, K2_LDS, stream>>>(Aq, wq, ws + WS_K, ws + WS_V, ws + WS_CTX, (const u16*)(ws + WS_UR), ssmk, ssmD, (u16*)(ws + WS_ZT) DBGPASS(1));
    k3_oproj<<<256, 512, K3_LDS, stream>>>(ws + WS_CTX, ws + WS_WT, Aq, (const u16*)(ws + WS_ZT), (const float*)(ws + WS_GATE), (u16*)d_out DBGPASS(2));
}
```
